```python
import torch

def _softmax(x, axis=-1):
    return torch.softmax(x, dim=axis)
def _gelu(x):
    return torch.nn.functional.gelu(x, approximate="tanh")
def _silu(x):
    return torch.nn.functional.silu(x)
def _sigmoid(x):
    return torch.sigmoid(x)
def _layernorm(x, axis=-1, eps=1e-5):
    m = x.mean(dim=axis, keepdim=True)
    v = x.var(dim=axis, keepdim=True, unbiased=False)
    return (x - m) / torch.sqrt(v + eps)
def _scatter_add(tgt, idx, src, axis=0):
    return tgt.clone().index_add_(axis, idx, src)
def _segment_sum(src, seg, n):
    out = torch.zeros((n,) + src.shape[1:], dtype=src.dtype)
    return out.index_add_(0, seg, src)
def _pad(x, pw):
    flat = tuple(v for pair in reversed(pw) for v in pair)
    return torch.nn.functional.pad(x, flat)

kernel_name = 'standalone__gqa_block_cached__near_ridge'

def setup_inputs():

    _w = (lambda a: (lambda t: t.to(torch.bfloat16) if t.is_floating_point() else t)(torch.as_tensor(a)))
    B, Sq, D = 8, 96, 512
    Hq, Hkv, Sk, Dh = 32, 32, 1024, 128
    assert Hq % Hkv == 0, (Hq, Hkv)
    return {
        "x": _w((lambda *s: torch.randn(*s, dtype=torch.bfloat16) * 0.8)(B, Sq, D)),
        "k_cache": _w((lambda *s: torch.randn(*s, dtype=torch.bfloat16) * 0.8)(B, Hkv, Sk, Dh)),
        "v_cache": _w((lambda *s: torch.randn(*s, dtype=torch.bfloat16) * 0.05)(B, Hkv, Sk, Dh)),
        "wq": _w((lambda *s: torch.randn(*s, dtype=torch.bfloat16) * 0.8)(D, Hq * Dh) * (D ** -0.5)),
        "wo": _w((lambda *s: torch.randn(*s, dtype=torch.bfloat16) * 0.05)(Hq * Dh, D) * ((Hq * Dh) ** -0.5)),
    }

def reference(x, k_cache, v_cache, wq, wo):
    B, Sq, D = x.shape
    Hq, Dh = 32, 128
    Hkv = k_cache.shape[1]
    rep = Hq // Hkv
    q = (lambda x, *d: x.permute(*d))((x @ wq).reshape(B, Sq, Hq, Dh), 0, 2, 1, 3)
    k = (lambda x, n, axis: torch.repeat_interleave(x, n, dim=axis))(k_cache, rep, axis=1)
    v = (lambda x, n, axis: torch.repeat_interleave(x, n, dim=axis))(v_cache, rep, axis=1)
    scores = torch.einsum("bhqd,bhkd->bhqk", q, k) * (Dh ** -0.5)
    p = _softmax(scores, axis=-1)
    o = torch.einsum("bhqk,bhkd->bhqd", p, v)
    return (lambda x, *d: x.permute(*d))(o, 0, 2, 1, 3).reshape(B, Sq, Hq * Dh) @ wo
```

```cpp
#include <hip/hip_runtime.h>
#include <hip/hip_bf16.h>

typedef short bf16x8 __attribute__((ext_vector_type(8)));
typedef short s16x4 __attribute__((ext_vector_type(4)));
typedef float f32x16 __attribute__((ext_vector_type(16)));
typedef float f32x4 __attribute__((ext_vector_type(4)));
typedef float f32x2 __attribute__((ext_vector_type(2)));
typedef __bf16 bf16x2_t __attribute__((ext_vector_type(2)));
typedef unsigned u32x4 __attribute__((ext_vector_type(4)));
typedef unsigned short u16;
#define LAS __attribute__((address_space(3)))
#define GAS __attribute__((address_space(1)))

constexpr int NB = 8, SQ = 96, DM = 512, NH = 32, SK = 1024, DH = 128, HD = NH * DH;
constexpr int TILE = 16384, NSLOT = 10, LDS_BYTES = NSLOT * TILE;
constexpr int NWQ = 8;
constexpr int NKV = SK / 64;
constexpr int NTILES = NWQ + 2 * NKV;
constexpr float QSCALE = 0.088388347648318440f * 1.4426950408889634f;
constexpr float THR = 8.0f;

#define KV_AUX 2

#define MFMA32(a, b, c) __builtin_amdgcn_mfma_f32_32x32x16_bf16((a), (b), (c), 0, 0, 0)
#define BAR() asm volatile("s_waitcnt lgkmcnt(0)\n\ts_barrier" ::: "memory")
#define SBAR() __builtin_amdgcn_sched_barrier(0)

#define STAMP(i)

__device__ __forceinline__ unsigned cvtpk(float lo, float hi) {
    f32x2 v = {lo, hi};
    return __builtin_bit_cast(unsigned, __builtin_convertvector(v, bf16x2_t));
}
__device__ __forceinline__ bf16x8 pack8(const f32x16& a, int base, float s) {
    u32x4 w = {cvtpk(a[base + 0] * s, a[base + 1] * s), cvtpk(a[base + 2] * s, a[base + 3] * s),
               cvtpk(a[base + 4] * s, a[base + 5] * s), cvtpk(a[base + 6] * s, a[base + 7] * s)};
    return __builtin_bit_cast(bf16x8, w);
}
__device__ __forceinline__ bf16x8 pack8n(const f32x16& a, int base) {
    u32x4 w = {cvtpk(a[base + 0], a[base + 1]), cvtpk(a[base + 2], a[base + 3]),
               cvtpk(a[base + 4], a[base + 5]), cvtpk(a[base + 6], a[base + 7])};
    return __builtin_bit_cast(bf16x8, w);
}
typedef short v4i16_t __attribute__((ext_vector_type(4)));
__device__ __forceinline__ bf16x8 trfrag(LAS const char* p) {
    v4i16_t lo = __builtin_amdgcn_ds_read_tr16_b64_v4i16((LAS v4i16_t*)p);
    v4i16_t hi = __builtin_amdgcn_ds_read_tr16_b64_v4i16((LAS v4i16_t*)(p + 256));
    return (bf16x8){lo[0], lo[1], lo[2], lo[3], hi[0], hi[1], hi[2], hi[3]};
}
__device__ __forceinline__ float max3f(float a, float b, float c) { return __builtin_fmaxf(__builtin_fmaxf(a, b), c); }
__device__ __forceinline__ float xhalf_max(float m) {
    auto rr = __builtin_amdgcn_permlane32_swap(__float_as_uint(m), __float_as_uint(m), false, false);
    return __builtin_fmaxf(__uint_as_float(rr[0]), __uint_as_float(rr[1]));
}
__device__ __forceinline__ float xhalf_sum(float m) {
    auto rr = __builtin_amdgcn_permlane32_swap(__float_as_uint(m), __float_as_uint(m), false, false);
    return __uint_as_float(rr[0]) + __uint_as_float(rr[1]);
}
template <int N> __device__ __forceinline__ void wait_vm() { asm volatile("s_waitcnt vmcnt(%0)" ::"i"(N) : "memory"); }

__global__ __launch_bounds__(512, 2) void attn_kernel(const u16* __restrict__ x, const u16* __restrict__ kc,
                                                      const u16* __restrict__ vc, const u16* __restrict__ wq,
                                                      u16* __restrict__ obuf) {
    extern __shared__ __attribute__((aligned(16))) char smem[];
    LAS char* lds = (LAS char*)smem;
    const int tid = threadIdx.x, lane = tid & 63;
    const int wave = __builtin_amdgcn_readfirstlane(tid >> 6);
    const int bid = blockIdx.x;
    const int head = (bid & 7) * 4 + ((bid >> 3) & 3), b = bid >> 5;
    if (wave == 0) STAMP(0);

    if (wave >= 4) {
        const int lw = wave - 4;
        const size_t kvoff = ((size_t)(b * NH + head)) * SK * DH;
        const __amdgpu_buffer_rsrc_t rk = __builtin_amdgcn_make_buffer_rsrc((void*)(kc + kvoff), 0, SK * DH * 2, 0x00020000);
        const __amdgpu_buffer_rsrc_t rv = __builtin_amdgcn_make_buffer_rsrc((void*)(vc + kvoff), 0, SK * DH * 2, 0x00020000);
        const __amdgpu_buffer_rsrc_t rw = __builtin_amdgcn_make_buffer_rsrc((void*)(wq + head * DH), 0, (DM * HD - head * DH) * 2, 0x00020000);
        int vk[4];
#pragma unroll
        for (int j = 0; j < 4; ++j) vk[j] = (4 * j + (lane >> 4)) * 256 + (((lane & 15) ^ (4 * j + (lane >> 4))) * 16);
        const int vv = ((lane >> 2) & 7) * 256 + (lane >> 5) * 64 + (lane & 3) * 16;
        const int vw = ((lane >> 2) & 7) * (HD * 2) + (lane >> 5) * 64 + (lane & 3) * 16;
        auto issue = [&](int n, int slot) {
            LAS char* dst = lds + slot * TILE + lw * 4096;
            if (n < NWQ) {
                const int so = (n * 64 + lw * 16) * (HD * 2);
#pragma unroll
                for (int j = 0; j < 4; ++j)
                    __builtin_amdgcn_raw_ptr_buffer_load_lds(rw, (LAS void*)(dst + j * 1024), 16, vw, so + (j >> 1) * 8 * (HD * 2) + (j & 1) * 128, 0, 0);
            } else {
                const int t = (n - NWQ) >> 1;
                const int so = t * TILE + lw * 4096;
                if (((n - NWQ) & 1) == 0) {
#pragma unroll
                    for (int j = 0; j < 4; ++j)
                        __builtin_amdgcn_raw_ptr_buffer_load_lds(rk, (LAS void*)(dst + j * 1024), 16, vk[j], so, 0, KV_AUX);
                } else {
#pragma unroll
                    for (int j = 0; j < 4; ++j)
                        __builtin_amdgcn_raw_ptr_buffer_load_lds(rv, (LAS void*)(dst + j * 1024), 16, vv, so + (j >> 1) * 2048 + (j & 1) * 128, 0, KV_AUX);
                }
            }
        };
#pragma unroll
        for (int n = 0; n < NSLOT; ++n) issue(n, n);
        wait_vm<4 * (NSLOT - 1)>();
        BAR();
        int slot = 0;
        for (int n = 1; n <= NTILES - NSLOT; ++n) {
            wait_vm<4 * (NSLOT - 2)>();
            BAR();
            issue(n - 1 + NSLOT, slot);
            slot = (slot == NSLOT - 1) ? 0 : slot + 1;
        }
        wait_vm<4 * 8>(); BAR();
        wait_vm<4 * 7>(); BAR();
        wait_vm<4 * 6>(); BAR();
        wait_vm<4 * 5>(); BAR();
        wait_vm<4 * 4>(); BAR();
        wait_vm<4 * 3>(); BAR();
        wait_vm<4 * 2>(); BAR();
        wait_vm<4 * 1>(); BAR();
        wait_vm<0>(); BAR();
        static_assert(NSLOT == 10, "tail ladder written for 10 slots");
        return;
    }
    if (wave == 3) {
        for (int n = 0; n < NTILES; ++n) BAR();
        return;
    }
    const int r = lane & 31, h = lane >> 5;
    const int qrow = b * SQ + wave * 32 + r;
    const int p4 = lane & 3, sw = ((p4 & 1) << 1) | (p4 >> 1);
    const int trb = h * 2048 + ((lane >> 2) & 3) * 64 + ((lane >> 4) & 1) * 32 + sw * 8;
    const int pr = (r & 19) | ((r & 4) << 1) | ((r & 8) >> 1);
    const int kb = pr * 256 + (((pr & 15) ^ h) * 16);

    bf16x8 qf[8];
    {
        bf16x8 xf[32];
        const u16* xr = x + (size_t)qrow * DM + 8 * h;
#pragma unroll
        for (int s = 0; s < 32; ++s) xf[s] = *(const bf16x8*)(xr + 16 * s);
        f32x16 qa[4] = {};
#pragma unroll
        for (int n = 0; n < NWQ; ++n) {
            BAR();
            LAS const char* tp = lds + n * TILE + trb;
#pragma unroll
            for (int ks = 0; ks < 4; ++ks)
#pragma unroll
                for (int t = 0; t < 4; ++t) qa[t] = MFMA32(trfrag(tp + ks * 4096 + t * 512), xf[4 * n + ks], qa[t]);
        }
#pragma unroll
        for (int t = 0; t < 4; ++t) { qf[2 * t] = pack8(qa[t], 0, QSCALE); qf[2 * t + 1] = pack8(qa[t], 8, QSCALE); }
    }
    if (wave == 0) STAMP(1);

    f32x16 o[4] = {};
    f32x16 negm = {};
    float l = 0.f;
    int slot = NWQ;
    for (int t = 0; t < NKV; ++t) {
        BAR();
        f32x16 s0, s1;
        {
            const int ka = kb + slot * TILE;
#pragma unroll
            for (int ks = 0; ks < 8; ++ks) {
                LAS const char* kp = lds + (ka ^ (ks * 32));
                bf16x8 k0 = *(LAS const bf16x8*)kp, k1 = *(LAS const bf16x8*)(kp + 8192);
                s0 = MFMA32(k0, qf[ks], ks == 0 ? negm : s0);
                s1 = MFMA32(k1, qf[ks], ks == 0 ? negm : s1);
            }
        }
        slot = (slot == NSLOT - 1) ? 0 : slot + 1;
        float a = max3f(s0[0], s0[1], s1[0]), c = max3f(s0[2], s0[3], s1[1]);
        a = max3f(a, s1[2], s1[3]);
#pragma unroll
        for (int i = 4; i < 16; i += 4) {
            a = max3f(a, s0[i], s0[i + 1]); c = max3f(c, s0[i + 2], s0[i + 3]);
            a = max3f(a, s1[i], s1[i + 1]); c = max3f(c, s1[i + 2], s1[i + 3]);
        }
        const float rm = xhalf_max(__builtin_fmaxf(a, c));
        if (t == 0 || __any(rm > THR)) {
            const float dl = (t == 0) ? rm : __builtin_fmaxf(rm, 0.f);
            const float al = (t == 0) ? 1.f : __builtin_amdgcn_exp2f(-dl);
            const float nm = negm[0] - dl;
#pragma unroll
            for (int i = 0; i < 16; ++i) { negm[i] = nm; s0[i] -= dl; s1[i] -= dl; }
            l *= al;
#pragma unroll
            for (int d = 0; d < 4; ++d)
#pragma unroll
                for (int i = 0; i < 16; ++i) o[d][i] *= al;
        }
        float ps0 = 0.f, ps1 = 0.f;
#pragma unroll
        for (int i = 0; i < 16; ++i) {
            s0[i] = __builtin_amdgcn_exp2f(s0[i]); ps0 += s0[i];
            s1[i] = __builtin_amdgcn_exp2f(s1[i]); ps1 += s1[i];
        }
        l += ps0 + ps1;
        bf16x8 pf[4] = {pack8n(s0, 0), pack8n(s0, 8), pack8n(s1, 0), pack8n(s1, 8)};
        BAR();
        {
            LAS const char* tp = lds + slot * TILE + trb;
#pragma unroll
            for (int ks = 0; ks < 4; ++ks)
#pragma unroll
                for (int d = 0; d < 4; ++d) o[d] = MFMA32(trfrag(tp + ks * 4096 + d * 512), pf[ks], o[d]);
        }
        slot = (slot == NSLOT - 1) ? 0 : slot + 1;
    }
    if (wave == 0) STAMP(2);
    const float inv = 1.0f / xhalf_sum(l);
    u16* orow = obuf + (size_t)qrow * HD + head * DH + 8 * h;
#pragma unroll
    for (int d = 0; d < 4; ++d) {
        *(bf16x8*)(orow + 32 * d) = pack8(o[d], 0, inv);
        *(bf16x8*)(orow + 32 * d + 16) = pack8(o[d], 8, inv);
    }
    if (wave == 0) { asm volatile("s_waitcnt vmcnt(0)" ::: "memory"); STAMP(3); }
}

__global__ __launch_bounds__(256) void oproj_kernel(const u16* __restrict__ obuf, const u16* __restrict__ wo, u16* __restrict__ out) {
    __shared__ float red[4][32 * 33];
    const int tid = threadIdx.x, lane = tid & 63, wave = tid >> 6, r = lane & 31, h = lane >> 5;
    const int rb = blockIdx.x >> 4, cb = blockIdx.x & 15;
    f32x16 acc = {};
    const u16* ap = obuf + (size_t)(rb * 32 + r) * HD + wave * 1024 + 8 * h;
    const u16* bp = wo + (size_t)(wave * 1024 + 8 * h) * DM + cb * 32 + r;
#pragma unroll 4
    for (int s = 0; s < 64; ++s) {
        bf16x8 a = *(const bf16x8*)(ap + 16 * s);
        bf16x8 bb;
#pragma unroll
        for (int j = 0; j < 8; ++j) bb[j] = (short)bp[(size_t)(16 * s + j) * DM];
        acc = MFMA32(a, bb, acc);
    }
#pragma unroll
    for (int i = 0; i < 16; ++i) red[wave][((i & 3) + 8 * (i >> 2) + 4 * h) * 33 + r] = acc[i];
    __syncthreads();
    for (int e = tid; e < 1024; e += 256) {
        const int row = e >> 5, col = e & 31;
        const float v = red[0][row * 33 + col] + red[1][row * 33 + col] + red[2][row * 33 + col] + red[3][row * 33 + col];
        out[(size_t)(rb * 32 + row) * DM + cb * 32 + col] = (u16)(cvtpk(v, v) & 0xffff);
    }
}

extern "C" void kernel_launch(void* const* d_in, const int* in_sizes, int n_in, void* d_out, int out_size, void* d_ws,
                              size_t ws_size, hipStream_t stream) {
    const u16* x = (const u16*)d_in[0];
    const u16* kc = (const u16*)d_in[1];
    const u16* vc = (const u16*)d_in[2];
    const u16* wq = (const u16*)d_in[3];
    const u16* wo = (const u16*)d_in[4];
    u16* obuf = (u16*)d_ws;
    hipFuncSetAttribute((const void*)attn_kernel, hipFuncAttributeMaxDynamicSharedMemorySize, LDS_BYTES);
    hipLaunchKernelGGL(attn_kernel, dim3(NB * NH), dim3(512), LDS_BYTES, stream, x, kc, vc, wq, obuf);
    hipLaunchKernelGGL(oproj_kernel, dim3(24 * 16), dim3(256), 0, stream, (const u16*)obuf, wo, (u16*)d_out);
}
```

```cpp
#include <hip/hip_runtime.h>
#include <hip/hip_bf16.h>

typedef short bf16x8 __attribute__((ext_vector_type(8)));
typedef short s16x4 __attribute__((ext_vector_type(4)));
typedef float f32x16 __attribute__((ext_vector_type(16)));
typedef float f32x4 __attribute__((ext_vector_type(4)));
typedef float f32x2 __attribute__((ext_vector_type(2)));
typedef __bf16 bf16x2_t __attribute__((ext_vector_type(2)));
typedef unsigned u32x4 __attribute__((ext_vector_type(4)));
typedef unsigned short u16;
#define LAS __attribute__((address_space(3)))
#define GAS __attribute__((address_space(1)))

constexpr int NB = 8, SQ = 96, DM = 512, NH = 32, SK = 1024, DH = 128, HD = NH * DH;
constexpr int TILE = 16384, NSLOT = 10, LDS_BYTES = NSLOT * TILE;
constexpr int NWQ = 8;
constexpr int NKV = SK / 64;
constexpr int NTILES = NWQ + 2 * NKV;
constexpr float QSCALE = 0.088388347648318440f * 1.4426950408889634f;
constexpr float THR = 8.0f;

#define KV_AUX 2

#define MFMA32(a, b, c) __builtin_amdgcn_mfma_f32_32x32x16_bf16((a), (b), (c), 0, 0, 0)
#define BAR() asm volatile("s_waitcnt lgkmcnt(0)\n\ts_barrier" ::: "memory")
#define SBAR() __builtin_amdgcn_sched_barrier(0)
#define PIN(x) asm volatile("" : "+v"(x))

#define STAMP(i)

__device__ __forceinline__ unsigned cvtpk(float lo, float hi) {
    f32x2 v = {lo, hi};
    return __builtin_bit_cast(unsigned, __builtin_convertvector(v, bf16x2_t));
}
__device__ __forceinline__ bf16x8 pack8(const f32x16& a, int base, float s) {
    u32x4 w = {cvtpk(a[base + 0] * s, a[base + 1] * s), cvtpk(a[base + 2] * s, a[base + 3] * s),
               cvtpk(a[base + 4] * s, a[base + 5] * s), cvtpk(a[base + 6] * s, a[base + 7] * s)};
    return __builtin_bit_cast(bf16x8, w);
}
__device__ __forceinline__ bf16x8 pack8n(const f32x16& a, int base) {
    u32x4 w = {cvtpk(a[base + 0], a[base + 1]), cvtpk(a[base + 2], a[base + 3]),
               cvtpk(a[base + 4], a[base + 5]), cvtpk(a[base + 6], a[base + 7])};
    return __builtin_bit_cast(bf16x8, w);
}
typedef short v4i16_t __attribute__((ext_vector_type(4)));
__device__ __forceinline__ bf16x8 trfrag(LAS const char* p) {
    v4i16_t lo = __builtin_amdgcn_ds_read_tr16_b64_v4i16((LAS v4i16_t*)p);
    v4i16_t hi = __builtin_amdgcn_ds_read_tr16_b64_v4i16((LAS v4i16_t*)(p + 256));
    return (bf16x8){lo[0], lo[1], lo[2], lo[3], hi[0], hi[1], hi[2], hi[3]};
}
__device__ __forceinline__ float max3f(float a, float b, float c) { return __builtin_fmaxf(__builtin_fmaxf(a, b), c); }
__device__ __forceinline__ float xhalf_max(float m) {
    auto rr = __builtin_amdgcn_permlane32_swap(__float_as_uint(m), __float_as_uint(m), false, false);
    return __builtin_fmaxf(__uint_as_float(rr[0]), __uint_as_float(rr[1]));
}
__device__ __forceinline__ float xhalf_sum(float m) {
    auto rr = __builtin_amdgcn_permlane32_swap(__float_as_uint(m), __float_as_uint(m), false, false);
    return __uint_as_float(rr[0]) + __uint_as_float(rr[1]);
}
template <int N> __device__ __forceinline__ void wait_vm() { asm volatile("s_waitcnt vmcnt(%0)" ::"i"(N) : "memory"); }

__global__ __launch_bounds__(512, 2) void attn_kernel(const u16* __restrict__ x, const u16* __restrict__ kc,
                                                      const u16* __restrict__ vc, const u16* __restrict__ wq,
                                                      u16* __restrict__ obuf) {
    extern __shared__ __attribute__((aligned(16))) char smem[];
    LAS char* lds = (LAS char*)smem;
    const int tid = threadIdx.x, lane = tid & 63;
    const int wave = __builtin_amdgcn_readfirstlane(tid >> 6);
    const int bid = blockIdx.x;
    const int head = (bid & 7) * 4 + ((bid >> 3) & 3), b = bid >> 5;
    if (wave == 0) STAMP(0);

    if (wave >= 4) {
        const int lw = wave - 4;
        const size_t kvoff = ((size_t)(b * NH + head)) * SK * DH;
        const __amdgpu_buffer_rsrc_t rk = __builtin_amdgcn_make_buffer_rsrc((void*)(kc + kvoff), 0, SK * DH * 2, 0x00020000);
        const __amdgpu_buffer_rsrc_t rv = __builtin_amdgcn_make_buffer_rsrc((void*)(vc + kvoff), 0, SK * DH * 2, 0x00020000);
        const __amdgpu_buffer_rsrc_t rw = __builtin_amdgcn_make_buffer_rsrc((void*)(wq + head * DH), 0, (DM * HD - head * DH) * 2, 0x00020000);
        int vk[4];
#pragma unroll
        for (int j = 0; j < 4; ++j) vk[j] = (4 * j + (lane >> 4)) * 256 + (((lane & 15) ^ (4 * j + (lane >> 4))) * 16);
        const int vv = ((lane >> 2) & 7) * 256 + (lane >> 5) * 64 + (lane & 3) * 16;
        const int vw = ((lane >> 2) & 7) * (HD * 2) + (lane >> 5) * 64 + (lane & 3) * 16;
        auto issue = [&](int n, int slot) {
            LAS char* dst = lds + slot * TILE + lw * 4096;
            if (n < NWQ) {
                const int so = (n * 64 + lw * 16) * (HD * 2);
#pragma unroll
                for (int j = 0; j < 4; ++j)
                    __builtin_amdgcn_raw_ptr_buffer_load_lds(rw, (LAS void*)(dst + j * 1024), 16, vw, so + (j >> 1) * 8 * (HD * 2) + (j & 1) * 128, 0, 0);
            } else {
                const int t = (n - NWQ) >> 1;
                const int so = t * TILE + lw * 4096;
                if (((n - NWQ) & 1) == 0) {
#pragma unroll
                    for (int j = 0; j < 4; ++j)
                        __builtin_amdgcn_raw_ptr_buffer_load_lds(rk, (LAS void*)(dst + j * 1024), 16, vk[j], so, 0, KV_AUX);
                } else {
#pragma unroll
                    for (int j = 0; j < 4; ++j)
                        __builtin_amdgcn_raw_ptr_buffer_load_lds(rv, (LAS void*)(dst + j * 1024), 16, vv, so + (j >> 1) * 2048 + (j & 1) * 128, 0, KV_AUX);
                }
            }
        };
#pragma unroll
        for (int n = 0; n < NSLOT; ++n) issue(n, n);
        if (wave == 4) STAMP(5);
        wait_vm<4 * (NSLOT - 1)>();
        if (wave == 4) STAMP(6);
        BAR();
        int slot = 0;
        for (int n = 1; n <= NTILES - NSLOT; ++n) {
            wait_vm<4 * (NSLOT - 2)>();
            BAR();
            issue(n - 1 + NSLOT, slot);
            slot = (slot == NSLOT - 1) ? 0 : slot + 1;
        }
        wait_vm<4 * 8>(); BAR();
        wait_vm<4 * 7>(); BAR();
        wait_vm<4 * 6>(); BAR();
        wait_vm<4 * 5>(); BAR();
        wait_vm<4 * 4>(); BAR();
        wait_vm<4 * 3>(); BAR();
        wait_vm<4 * 2>(); BAR();
        wait_vm<4 * 1>(); BAR();
        wait_vm<0>(); BAR();
        static_assert(NSLOT == 10, "tail ladder written for 10 slots");
        return;
    }
    if (wave == 3) return;
    const int r = lane & 31, h = lane >> 5;
    const int qrow = b * SQ + wave * 32 + r;
    const int p4 = lane & 3, sw = ((p4 & 1) << 1) | (p4 >> 1);
    const int trb = h * 2048 + ((lane >> 2) & 3) * 64 + ((lane >> 4) & 1) * 32 + sw * 8;
    const int pr = (r & 19) | ((r & 4) << 1) | ((r & 8) >> 1);
    const int kb = pr * 256 + (((pr & 15) ^ h) * 16);

    bf16x8 qf[8];
    {
        bf16x8 xf[32];
        const u16* xr = x + (size_t)qrow * DM + 8 * h;
#pragma unroll
        for (int s = 0; s < 32; ++s) xf[s] = *(const bf16x8*)(xr + 16 * s);
        f32x16 qa[4] = {};
        bf16x8 fa[4], fb[4], fc[4];
        BAR();
        if (wave == 0) STAMP(4);
#define RD4(F, n, ks) do { _Pragma("unroll") for (int t_ = 0; t_ < 4; ++t_) F[t_] = trfrag(lds + (n) * TILE + trb + (ks) * 4096 + t_ * 512); SBAR(); } while (0)
#define MM4(F, X) do { _Pragma("unroll") for (int t_ = 0; t_ < 4; ++t_) qa[t_] = MFMA32(F[t_], X, qa[t_]); SBAR(); } while (0)
        RD4(fa, 0, 0); RD4(fb, 0, 1);
#pragma unroll
        for (int g = 0; g < 32; ++g) {
            const int gn = g + 2;
            if (gn < 32) {
                if ((gn & 3) == 0) { BAR(); }
                if (gn % 3 == 0) RD4(fa, gn >> 2, gn & 3); else if (gn % 3 == 1) RD4(fb, gn >> 2, gn & 3); else RD4(fc, gn >> 2, gn & 3);
            }
            if (g % 3 == 0) MM4(fa, xf[g]); else if (g % 3 == 1) MM4(fb, xf[g]); else MM4(fc, xf[g]);
        }
#undef RD4
#undef MM4
#pragma unroll
        for (int t = 0; t < 4; ++t) { qf[2 * t] = pack8(qa[t], 0, QSCALE); qf[2 * t + 1] = pack8(qa[t], 8, QSCALE); }
    }
    if (wave == 0) STAMP(1);

    f32x16 o[4] = {};
    f32x16 negm = {};
    float l = 0.f;
    int slot = NWQ;
    for (int t = 0; t < NKV; ++t) {
        BAR();
        f32x16 s0, s1;
        bf16x8 fr[16];
        {
            const int ka = kb + slot * TILE;
#pragma unroll
            for (int ks = 0; ks < 8; ++ks) {
                LAS const char* kp = lds + (ka ^ (ks * 32));
                fr[2 * ks] = *(LAS const bf16x8*)kp; fr[2 * ks + 1] = *(LAS const bf16x8*)(kp + 8192);
            }
            SBAR();
#pragma unroll
            for (int ks = 0; ks < 8; ++ks) {
                s0 = MFMA32(fr[2 * ks], qf[ks], ks == 0 ? negm : s0);
                s1 = MFMA32(fr[2 * ks + 1], qf[ks], ks == 0 ? negm : s1);
            }
            PIN(s0); PIN(s1);
            SBAR();
        }
        slot = (slot == NSLOT - 1) ? 0 : slot + 1;
        BAR();
        {
            LAS const char* tp = lds + slot * TILE + trb;
#pragma unroll
            for (int ks = 0; ks < 4; ++ks)
#pragma unroll
                for (int d = 0; d < 4; ++d) fr[ks * 4 + d] = trfrag(tp + ks * 4096 + d * 512);
            asm volatile("" ::: "memory");
            SBAR();
        }
        slot = (slot == NSLOT - 1) ? 0 : slot + 1;
        float a = max3f(s0[0], s0[1], s1[0]), c = max3f(s0[2], s0[3], s1[1]);
        a = max3f(a, s1[2], s1[3]);
#pragma unroll
        for (int i = 4; i < 16; i += 4) {
            a = max3f(a, s0[i], s0[i + 1]); c = max3f(c, s0[i + 2], s0[i + 3]);
            a = max3f(a, s1[i], s1[i + 1]); c = max3f(c, s1[i + 2], s1[i + 3]);
        }
        const float rm = xhalf_max(__builtin_fmaxf(a, c));
        if (t == 0 || __any(rm > THR)) {
            const float dl = (t == 0) ? rm : __builtin_fmaxf(rm, 0.f);
            const float al = (t == 0) ? 1.f : __builtin_amdgcn_exp2f(-dl);
            const float nm = negm[0] - dl;
#pragma unroll
            for (int i = 0; i < 16; ++i) { negm[i] = nm; s0[i] -= dl; s1[i] -= dl; }
            l *= al;
#pragma unroll
            for (int d = 0; d < 4; ++d)
#pragma unroll
                for (int i = 0; i < 16; ++i) o[d][i] *= al;
        }
        float ps0 = 0.f, ps1 = 0.f;
#pragma unroll
        for (int i = 0; i < 16; ++i) { s0[i] = __builtin_amdgcn_exp2f(s0[i]); ps0 += s0[i]; }
#pragma unroll
        for (int i = 0; i < 16; ++i) { s1[i] = __builtin_amdgcn_exp2f(s1[i]); ps1 += s1[i]; }
        l += ps0 + ps1;
        bf16x8 pf[4] = {pack8n(s0, 0), pack8n(s0, 8), pack8n(s1, 0), pack8n(s1, 8)};
#pragma unroll
        for (int ks = 0; ks < 4; ++ks)
#pragma unroll
            for (int d = 0; d < 4; ++d) o[d] = MFMA32(fr[ks * 4 + d], pf[ks], o[d]);
        PIN(o[0]); PIN(o[1]); PIN(o[2]); PIN(o[3]); PIN(l);
        SBAR();
    }
    if (wave == 0) STAMP(2);
    const float inv = 1.0f / xhalf_sum(l);
    u16* orow = obuf + (size_t)qrow * HD + head * DH + 8 * h;
#pragma unroll
    for (int d = 0; d < 4; ++d) {
        *(bf16x8*)(orow + 32 * d) = pack8(o[d], 0, inv);
        *(bf16x8*)(orow + 32 * d + 16) = pack8(o[d], 8, inv);
    }
    if (wave == 0) { asm volatile("s_waitcnt vmcnt(0)" ::: "memory"); STAMP(3); }
}

__global__ __launch_bounds__(256) void oproj_kernel(const u16* __restrict__ obuf, const u16* __restrict__ wo, u16* __restrict__ out) {
    __shared__ float red[4][32 * 33];
    const int tid = threadIdx.x, lane = tid & 63, wave = tid >> 6, r = lane & 31, h = lane >> 5;
    const int rb = blockIdx.x >> 4, cb = blockIdx.x & 15;
    f32x16 acc = {};
    const u16* ap = obuf + (size_t)(rb * 32 + r) * HD + wave * 1024 + 8 * h;
    const u16* bp = wo + (size_t)(wave * 1024 + 8 * h) * DM + cb * 32 + r;
#pragma unroll 4
    for (int s = 0; s < 64; ++s) {
        bf16x8 a = *(const bf16x8*)(ap + 16 * s);
        bf16x8 bb;
#pragma unroll
        for (int j = 0; j < 8; ++j) bb[j] = (short)bp[(size_t)(16 * s + j) * DM];
        acc = MFMA32(a, bb, acc);
    }
#pragma unroll
    for (int i = 0; i < 16; ++i) red[wave][((i & 3) + 8 * (i >> 2) + 4 * h) * 33 + r] = acc[i];
    __syncthreads();
    for (int e = tid; e < 1024; e += 256) {
        const int row = e >> 5, col = e & 31;
        const float v = red[0][row * 33 + col] + red[1][row * 33 + col] + red[2][row * 33 + col] + red[3][row * 33 + col];
        out[(size_t)(rb * 32 + row) * DM + cb * 32 + col] = (u16)(cvtpk(v, v) & 0xffff);
    }
}

extern "C" void kernel_launch(void* const* d_in, const int* in_sizes, int n_in, void* d_out, int out_size, void* d_ws,
                              size_t ws_size, hipStream_t stream) {
    const u16* x = (const u16*)d_in[0];
    const u16* kc = (const u16*)d_in[1];
    const u16* vc = (const u16*)d_in[2];
    const u16* wq = (const u16*)d_in[3];
    const u16* wo = (const u16*)d_in[4];
    u16* obuf = (u16*)d_ws;
    (void)hipFuncSetAttribute((const void*)attn_kernel, hipFuncAttributeMaxDynamicSharedMemorySize, LDS_BYTES);
    hipLaunchKernelGGL(attn_kernel, dim3(NB * NH), dim3(512), LDS_BYTES, stream, x, kc, vc, wq, obuf);
    hipLaunchKernelGGL(oproj_kernel, dim3(24 * 16), dim3(256), 0, stream, (const u16*)obuf, wo, (u16*)d_out);
}
```

```cpp
#include <hip/hip_runtime.h>
#include <hip/hip_bf16.h>

typedef short bf16x8 __attribute__((ext_vector_type(8)));
typedef float f32x16 __attribute__((ext_vector_type(16)));
typedef float f32x2 __attribute__((ext_vector_type(2)));
typedef __bf16 bf16x2_t __attribute__((ext_vector_type(2)));
typedef unsigned u32x4 __attribute__((ext_vector_type(4)));
typedef unsigned u32x2 __attribute__((ext_vector_type(2)));
typedef short v4i16_t __attribute__((ext_vector_type(4)));
typedef unsigned short u16;
typedef unsigned long long u64;
#define LAS __attribute__((address_space(3)))
#define GAS __attribute__((address_space(1)))

constexpr int NB = 8, SQ = 96, DM = 512, NH = 32, SK = 1024, DH = 128, HD = NH * DH;
constexpr int TILE = 16384;
constexpr int NSL = 9;
constexpr int LDS_F = NSL * TILE;
constexpr int LDS_BYTES = LDS_F + 512;
constexpr int NWQ = 8;
#define NWS 6
#define WIN 7
constexpr int wslot(int i) { return NSL - NWS + (i + 8 * NWS - NWQ) % NWS; }
constexpr int NKV = SK / 64;
constexpr int NST = 2 * NKV;
constexpr float QSCALE = 0.088388347648318440f * 1.4426950408889634f;
constexpr float THR = 8.0f;

#define XEARLY 3
#define INFL 3
#define TRIG 1
#define WLAG 4

#define WOTRIG 31
constexpr int WS_EPOCH = 0;
constexpr int WS_FO = 4096;
constexpr int WS_FP = 16384;
constexpr int WS_O = 65536;
constexpr int WS_P = WS_O + NB * NH * 24576;
constexpr int WS_END = WS_P + NB * 4 * 64 * 3072;
constexpr unsigned SPIN_CAP = 1u << 20;
#define MFMA32(a, b, c) __builtin_amdgcn_mfma_f32_32x32x16_bf16((a), (b), (c), 0, 0, 0)
#define BAR() asm volatile("s_waitcnt lgkmcnt(0)\n\ts_barrier" ::: "memory")
#define SBAR() __builtin_amdgcn_sched_barrier(0)
#define PIN(x) asm volatile("" : "+v"(x))
#define MEMFENCE() asm volatile("" ::: "memory")

#define RT(v)
#define PUT(i, v)

__device__ __forceinline__ unsigned cvtpk(float lo, float hi) {
    f32x2 v = {lo, hi};
    return __builtin_bit_cast(unsigned, __builtin_convertvector(v, bf16x2_t));
}
__device__ __forceinline__ bf16x8 pack8(const f32x16& a, int base, float s) {
    u32x4 w = {cvtpk(a[base + 0] * s, a[base + 1] * s), cvtpk(a[base + 2] * s, a[base + 3] * s),
               cvtpk(a[base + 4] * s, a[base + 5] * s), cvtpk(a[base + 6] * s, a[base + 7] * s)};
    return __builtin_bit_cast(bf16x8, w);
}
__device__ __forceinline__ bf16x8 trfrag(LAS const char* p) {
    v4i16_t lo = __builtin_amdgcn_ds_read_tr16_b64_v4i16((LAS v4i16_t*)p);
    v4i16_t hi = __builtin_amdgcn_ds_read_tr16_b64_v4i16((LAS v4i16_t*)(p + 256));
    return (bf16x8){lo[0], lo[1], lo[2], lo[3], hi[0], hi[1], hi[2], hi[3]};
}
__device__ __forceinline__ float max3f(float a, float b, float c) { return __builtin_fmaxf(__builtin_fmaxf(a, b), c); }
__device__ __forceinline__ float xhalf_max(float m) {
    auto rr = __builtin_amdgcn_permlane32_swap(__float_as_uint(m), __float_as_uint(m), false, false);
    return __builtin_fmaxf(__uint_as_float(rr[0]), __uint_as_float(rr[1]));
}
__device__ __forceinline__ float xhalf_sum(float m) {
    auto rr = __builtin_amdgcn_permlane32_swap(__float_as_uint(m), __float_as_uint(m), false, false);
    return __uint_as_float(rr[0]) + __uint_as_float(rr[1]);
}
template <int I> struct IC { static constexpr int v = I; };
template <int I, int N, class F> __device__ __forceinline__ void static_for(F&& f) {
    if constexpr (I < N) { f(IC<I>{}); static_for<I + 1, N>(f); }
}
template <int N> __device__ __forceinline__ void wait_vm() { asm volatile("s_waitcnt vmcnt(%0)" ::"i"(N) : "memory"); }
__device__ __forceinline__ unsigned lds_ld(LAS const char* p) { return __builtin_amdgcn_readfirstlane(*(volatile LAS const unsigned*)p); }
__device__ __forceinline__ void lds_st(LAS char* p, unsigned v) { MEMFENCE(); *(volatile LAS unsigned*)p = v; MEMFENCE(); }
__device__ __forceinline__ unsigned min3w(LAS const char* p) {
    const u32x4 v = *(volatile LAS const u32x4*)p;
    const unsigned m = v[0] < v[1] ? v[0] : v[1];
    return __builtin_amdgcn_readfirstlane(m < v[2] ? m : v[2]);
}
__device__ __forceinline__ unsigned min4v(u32x4 v) {
    const unsigned m = v[0] < v[1] ? v[0] : v[1], n = v[2] < v[3] ? v[2] : v[3];
    return __builtin_amdgcn_readfirstlane(m < n ? m : n);
}
__device__ __forceinline__ void wait4(LAS const char* p, unsigned& cached, unsigned need) {
    MEMFENCE();
    while (cached < need) cached = min4v(*(volatile LAS const u32x4*)p);
    MEMFENCE();
}
__device__ __forceinline__ void wait1(LAS const char* p, unsigned& cached, unsigned need) {
    MEMFENCE();
    while (cached < need) cached = lds_ld(p);
    MEMFENCE();
}
__device__ __forceinline__ u64 ld_agent(const char* p) { return __hip_atomic_load((GAS const u64*)p, __ATOMIC_RELAXED, __HIP_MEMORY_SCOPE_AGENT); }
__device__ __forceinline__ void st_agent(char* p, u64 v) { __hip_atomic_store((GAS u64*)p, v, __ATOMIC_RELAXED, __HIP_MEMORY_SCOPE_AGENT); }
__device__ __forceinline__ void poll_tags(const char* p, u64 tag) {
    for (unsigned spins = 0; spins < SPIN_CAP; ++spins) {
        if (__all(ld_agent(p) == tag)) break;
        __builtin_amdgcn_s_sleep(1);
    }
    __builtin_amdgcn_fence(__ATOMIC_ACQUIRE, "wavefront");
    MEMFENCE();
}
__device__ __forceinline__ void st_sc1(bf16x8 v, __amdgpu_buffer_rsrc_t rs, int vo, int so) {
    __builtin_amdgcn_raw_buffer_store_b128(__builtin_bit_cast(u32x4, v), rs, vo, so, 16);
    asm volatile("s_nop 1" :: "v"(v) : "memory");
}
#define DMA(rs, dst, vo, so, imm, aux) __builtin_amdgcn_raw_ptr_buffer_load_lds((rs), (LAS void*)(dst), 16, (vo), (so), (imm), (aux))

__global__ __launch_bounds__(512, 2) void attn_kernel(const u16* __restrict__ x, const u16* __restrict__ kc,
                                                      const u16* __restrict__ vc, const u16* __restrict__ wq,
                                                      const u16* __restrict__ wo, u16* __restrict__ out, char* ws) {
    extern __shared__ __attribute__((aligned(16))) char smem[];
    LAS char* lds = (LAS char*)smem;
    LAS char* f_prog = lds + LDS_F;
    LAS char* f_wq = lds + LDS_F + 16;
    LAS char* f_kv = lds + LDS_F + 32;
    LAS char* f_go = lds + LDS_F + 36;
    LAS char* f_cnt = lds + LDS_F + 40;
    LAS char* f_tag = lds + LDS_F + 48;
    const int tid = threadIdx.x, lane = tid & 63;
    const int wave = __builtin_amdgcn_readfirstlane(tid >> 6);
    const int bid = blockIdx.x;
    const int grp = bid & 7, mem = (bid >> 3) & 3, head = grp * 4 + mem, b = bid >> 5;
    const int rot = b + 2 * (head & 3);
    [[maybe_unused]] unsigned long long t0 = 0, t1 = 0, t2 = 0, t3 = 0, t4 = 0;
    RT(t0);

    if (wave == 3) {
        constexpr int BIAS = 2048;
        const size_t kvoff = ((size_t)(b * NH + head)) * SK * DH;
        const __amdgpu_buffer_rsrc_t rk = __builtin_amdgcn_make_buffer_rsrc((char*)(kc + kvoff) - BIAS, 0, SK * DH * 2 + BIAS, 0x00020000);
        const __amdgpu_buffer_rsrc_t rv = __builtin_amdgcn_make_buffer_rsrc((char*)(vc + kvoff) - BIAS, 0, SK * DH * 2 + BIAS, 0x00020000);
        int vk[4], vv[2];
#pragma unroll
        for (int j = 0; j < 4; ++j)
            vk[j] = (((lane >> 4) & 1) * 8 + ((lane >> 2) & 3)) * 256 + (lane >> 5) * 64 + (((lane & 3) ^ ((j & 2) + ((lane >> 4) & 1))) * 16)
                    + BIAS + (j >> 1) * 1024 + (j & 1) * 128 - j * 1024;
#pragma unroll
        for (int j = 0; j < 2; ++j) vv[j] = ((lane >> 2) & 7) * 256 + (lane >> 5) * 64 + (lane & 3) * 16 + BIAS + j * (128 - 1024);
        if (lane < 16) ((LAS unsigned*)(lds + LDS_F))[lane] = 0u;
        BAR();
        while (lds_ld(f_go) == 0u) __builtin_amdgcn_s_sleep(2);
        int pub = 0, lo = 0; unsigned P = 0;
        auto publish = [&](int n) {
            if (n > pub) {
                pub = n; lds_st(f_kv, (unsigned)pub);
            }
        };
        for (int j = 0; j < NST; ++j) {
            const unsigned need = j < NSL - NWS ? 0u : (unsigned)j;
            if (P < need) {
                P = min3w(f_prog);
                if (P < need) {
                    wait_vm<0>(); publish(j);
                    while ((P = min3w(f_prog)) < need) __builtin_amdgcn_s_sleep(1);
                }
            }
            MEMFENCE();
            LAS char* dst = lds + lo;
            const bool isk = (j == 0) || ((j & 1) && j != 31);
            const int so = (isk ? (j + 1) >> 1 : (j == 31 ? 15 : (j >> 1) - 1)) * TILE;
            const __amdgpu_buffer_rsrc_t rs = isk ? rk : rv;
            const int v0 = isk ? vk[0] : vv[0], v1 = isk ? vk[1] : vv[1], v2 = isk ? vk[2] : vv[0], v3 = isk ? vk[3] : vv[1];
#pragma unroll
            for (int q = 0; q < 4; ++q) {
                DMA(rs, dst + q * 4096, v0, so + q * 4096, 0, 2); DMA(rs, dst + q * 4096, v1, so + q * 4096, 1024, 2);
                DMA(rs, dst + q * 4096, v2, so + q * 4096, 2048, 2); DMA(rs, dst + q * 4096, v3, so + q * 4096, 3072, 2);
                wait_vm<4 * (WIN - 1)>();
                if (((q + 1 + 64 - (WIN - 1)) & 3) == 0) publish((4 * j + q + 1 - (WIN - 1)) >> 2);
            }
            lo = (lo == (NSL - 1) * TILE) ? 0 : lo + TILE;
            if (j == WOTRIG) lds_st(f_go, 2u);
        }
        static_for<0, WIN - 1>([&](auto ic) {
            constexpr int left = WIN - 2 - decltype(ic)::v;
            if constexpr ((left & 3) == 0) { wait_vm<4 * left>(); publish(NST - left / 4); }
        });
        RT(t1); PUT(5, t1);
        return;
    }
    if (wave >= 4) {
        const int lw = wave - 4;
        const __amdgpu_buffer_rsrc_t rw = __builtin_amdgcn_make_buffer_rsrc((void*)(wq + head * DH), 0, (DM * HD - head * DH) * 2, 0x00020000);
        const int vw = ((lane >> 2) & 7) * (HD * 2) + (lane >> 5) * 64 + (lane & 3) * 16;
        LAS char* dst = lds + lw * 4096;
        LAS char* myf = f_wq + lw * 4;
        auto dma = [&](int i) {
            const int so = (((i + rot) & 7) * 64 + lw * 16) * (HD * 2);
#pragma unroll
            for (int p = 0; p < 4; ++p) DMA(rw, dst + wslot(i) * TILE + p * 1024, vw, so + (p >> 1) * 8 * (HD * 2) + (p & 1) * 128, 0, 0);
        };
        static_for<0, NWS>([&](auto ic) {
            constexpr int i = decltype(ic)::v;
            dma(i);
            if constexpr (i == 0) { BAR(); wait_vm<0>(); lds_st(myf, 1u); RT(t1); }
            if constexpr (i == TRIG) { if (lw == 0) lds_st(f_go, 1u); }
        });
        RT(t2);
        static_for<1, NWS>([&](auto ic) { constexpr int i = decltype(ic)::v; wait_vm<4 * (NWS - 1 - i)>(); lds_st(myf, (unsigned)(i + 1)); });
        static_for<NWS, NWQ>([&](auto ic) {
            constexpr int i = decltype(ic)::v;
            while (min3w(f_prog) < (unsigned)(i - NWS + 1)) __builtin_amdgcn_s_sleep(1);
            MEMFENCE();
            dma(i);
        });
        static_for<NWS, NWQ>([&](auto ic) { constexpr int i = decltype(ic)::v; wait_vm<4 * (NWQ - 1 - i)>(); lds_st(myf, (unsigned)(i + 1)); });
        u64 tag = 0;
        if (lw == 3) {
            tag = ld_agent(ws + WS_EPOCH) * 0x9E3779B97F4A7C15ull + 0x632BE59BD9B4E019ull;
            *(volatile LAS u64*)f_tag = tag;
        }
        const __amdgpu_buffer_rsrc_t ro = __builtin_amdgcn_make_buffer_rsrc((void*)(wo + (size_t)grp * 512 * DM + mem * 128), 0, (512 * DM - mem * 128) * 2, 0x00020000);
        const int vo = ((lane >> 2) & 7) * (DM * 2) + (lane >> 5) * 64 + (lane & 3) * 16;
        while (lds_ld(f_go) < 2u) __builtin_amdgcn_s_sleep(8);
        RT(t1);
        unsigned P = 0;
        static_for<0, 8>([&](auto ic) {
            constexpr int m = decltype(ic)::v;
            while (P < (unsigned)(NST + m)) { P = min3w(f_prog); if (P < (unsigned)(NST + m)) __builtin_amdgcn_s_sleep(1); }
            MEMFENCE();
            const int so = ((((mem + (m >> 1)) & 3) * 2 + (m & 1)) * 64 + lw * 16) * (DM * 2);
#pragma unroll
            for (int p = 0; p < 4; ++p) DMA(ro, dst + ((NST + m) % NSL) * TILE + p * 1024, vo, so + (p >> 1) * 8 * (DM * 2) + (p & 1) * 128, 0, 0);
            if constexpr (m == 3) { wait_vm<8>(); lds_st(myf, (unsigned)(NWQ + 2)); }
        });
        wait_vm<0>(); lds_st(myf, (unsigned)(NWQ + 8));
        RT(t2);
        if (wave == 4) { PUT(8, t1); PUT(9, t2); }
        if (bid == 0 && lw == 3) {
#pragma unroll
            for (int k = 0; k < 4; ++k) poll_tags(ws + WS_FP + (k * 8 + (lane >> 3)) * 128 + (lane & 7) * 8, tag);
            if (lane == 0) st_agent(ws + WS_EPOCH, tag);
        }
        return;
    }
    const int r = lane & 31, h = lane >> 5;
    const int p4 = lane & 3, sw = ((p4 & 1) << 1) | (p4 >> 1);
    const int trb = h * 2048 + ((lane >> 2) & 3) * 64 + ((lane >> 4) & 1) * 32 + sw * 8;
    const int kb0 = (r >> 3) * 2048 + (r & 7) * 64 + ((h ^ ((r >> 2) & 3)) * 16), kb1 = kb0 ^ 32;
    LAS char* myprog = f_prog + wave * 4;

    bf16x8 qf[8];
    {
        const __amdgpu_buffer_rsrc_t rx = __builtin_amdgcn_make_buffer_rsrc((void*)(x + (size_t)(b * SQ + wave * 32) * DM), 0, 32 * DM * 2, 0x00020000);
        const int vx = ((lane >> 2) & 7) * (DM * 2) + (lane >> 5) * 64 + (lane & 3) * 16;
        u32x4 xr[NWQ][4];
#define XSEL(Q, M, d0, k0, o0, d1, k1, o1, d2, k2, o2, d3, k3, o3) \
        asm volatile("s_mov_b64 vcc, %12\n\t" \
            "v_cndmask_b32_dpp %0, %5, %4, vcc quad_perm:" Q " row_mask:0xf bank_mask:0xf\n\t" \
            "v_cndmask_b32_dpp %1, %7, %6, vcc quad_perm:" Q " row_mask:0xf bank_mask:0xf\n\t" \
            "v_cndmask_b32_dpp %2, %9, %8, vcc quad_perm:" Q " row_mask:0xf bank_mask:0xf\n\t" \
            "v_cndmask_b32_dpp %3, %11, %10, vcc quad_perm:" Q " row_mask:0xf bank_mask:0xf" \
            : "=&v"(d0), "=&v"(d1), "=&v"(d2), "=&v"(d3) \
            : "v"(k0), "v"(o0), "v"(k1), "v"(o1), "v"(k2), "v"(o2), "v"(k3), "v"(o3), "s"(M) : "vcc")
        u32x4 xt[4];
        auto xstep = [&](int i, int st) {
            const unsigned long long E0 = 0x5555555555555555ull, O0 = 0xAAAAAAAAAAAAAAAAull, E1 = 0x3333333333333333ull, O1 = 0xCCCCCCCCCCCCCCCCull;
            u32x4* X = xr[i];
            if (st == 0) XSEL("[1,0,3,2]", E0, xt[0][0], X[0][0], X[1][0], xt[0][1], X[0][1], X[1][1], xt[0][2], X[0][2], X[1][2], xt[0][3], X[0][3], X[1][3]);
            if (st == 1) XSEL("[1,0,3,2]", O0, xt[1][0], X[1][0], X[0][0], xt[1][1], X[1][1], X[0][1], xt[1][2], X[1][2], X[0][2], xt[1][3], X[1][3], X[0][3]);
            if (st == 2) XSEL("[1,0,3,2]", E0, xt[2][0], X[2][0], X[3][0], xt[2][1], X[2][1], X[3][1], xt[2][2], X[2][2], X[3][2], xt[2][3], X[2][3], X[3][3]);
            if (st == 3) XSEL("[1,0,3,2]", O0, xt[3][0], X[3][0], X[2][0], xt[3][1], X[3][1], X[2][1], xt[3][2], X[3][2], X[2][2], xt[3][3], X[3][3], X[2][3]);
            if (st == 4) XSEL("[2,3,0,1]", E1, X[0][0], xt[0][0], xt[2][0], X[0][1], xt[0][1], xt[2][1], X[0][2], xt[0][2], xt[2][2], X[0][3], xt[0][3], xt[2][3]);
            if (st == 5) XSEL("[2,3,0,1]", O1, X[2][0], xt[2][0], xt[0][0], X[2][1], xt[2][1], xt[0][1], X[2][2], xt[2][2], xt[0][2], X[2][3], xt[2][3], xt[0][3]);
            if (st == 6) XSEL("[2,3,0,1]", E1, X[1][0], xt[1][0], xt[3][0], X[1][1], xt[1][1], xt[3][1], X[1][2], xt[1][2], xt[3][2], X[1][3], xt[1][3], xt[3][3]);
            if (st == 7) XSEL("[2,3,0,1]", O1, X[3][0], xt[3][0], xt[1][0], X[3][1], xt[3][1], xt[1][1], X[3][2], xt[3][2], xt[1][2], X[3][3], xt[3][3], xt[1][3]);
        };
        static_for<0, NWQ>([&](auto ic) {
            constexpr int i = decltype(ic)::v;
            const int so = ((i + rot) & 7) * 128;
#pragma unroll
            for (int u = 0; u < 4; ++u) xr[i][u] = __builtin_amdgcn_raw_buffer_load_b128(rx, vx, so + u * 8 * (DM * 2), 0);
            SBAR();
            if constexpr (i == 1) BAR();
        });
        RT(t1);
#pragma unroll
        for (int i = 0; i < XEARLY; ++i)
#pragma unroll
            for (int st = 0; st < 8; ++st) xstep(i, st);
        f32x16 qa[4] = {};
        bf16x8 fw[8];
        int wb0 = trb + h * 6144 + TILE, wb1 = wb0 + 4 * TILE; PIN(wb0); PIN(wb1);
#define WRD(n) (fw[(n) & 7] = trfrag(lds + (wslot((n) >> 4) < 5 ? wb0 : wb1) + ((wslot((n) >> 4) - 1) & 3) * TILE + (((n) >> 2) & 3) * 2048 + ((n) & 3) * 512))
        unsigned wl = 0; u32x4 pf4;
        wait4(f_wq, wl, 1u);
        RT(t2);
#pragma unroll
        for (int n = 0; n < 8; ++n) WRD(n);
        MEMFENCE(); SBAR();
        static_for<0, 128>([&](auto ic) {
            constexpr int n = decltype(ic)::v;
            qa[n & 3] = MFMA32(fw[n & 7], __builtin_bit_cast(bf16x8, xr[n >> 4][(n >> 2) & 3]), qa[n & 3]);
            PIN(qa[n & 3]);
            constexpr int nn = n + 8, ti = n >> 4, ph = n & 15;
            if constexpr (nn < 128) {
                if constexpr ((nn & 15) == 0) {
                    lds_st(myprog, (unsigned)(nn >> 4));
                    wl = min4v(pf4);
                    wait4(f_wq, wl, (unsigned)((nn >> 4) + 1));
                }
                WRD(nn); MEMFENCE();
                if constexpr (nn == 127) lds_st(myprog, (unsigned)NWQ);
            }
            constexpr int SPX = (16 * (NWQ - 1)) / (8 * (NWQ - XEARLY)), xs_ = n / SPX;
            if constexpr (n % SPX == 0 && xs_ < 8 * (NWQ - XEARLY)) { static_assert((XEARLY + xs_ / 8) * 16 > n + 2, "late"); xstep(XEARLY + xs_ / 8, xs_ % 8); }
            if constexpr (ph == 3 && ti + 1 < NWQ) { pf4 = *(volatile LAS const u32x4*)f_wq; MEMFENCE(); }
            SBAR();
        });
#undef WRD
#pragma unroll
        for (int t = 0; t < 4; ++t) { qf[2 * t] = pack8(qa[t], 0, QSCALE); qf[2 * t + 1] = pack8(qa[t], 8, QSCALE); }
    }
    RT(t3);

    f32x16 o[4] = {};
    f32x16 negm, s0, s1;
    float l = 0.f, lb = 0.f;
    unsigned kvl = 0;
    unsigned pw[16];
    bf16x8 fr[8];
    int so = 0, jt = 0;
#define NEXT_TILE() do { ++jt; lds_st(myprog, (unsigned)(NWQ + jt)); so = (so == (NSL - 1) * TILE) ? 0 : so + TILE; } while (0)
#define WAIT_TILE() wait1(f_kv, kvl, (unsigned)(jt + 1))
#define KRD(i) (fr[(i) & 7] = *(LAS const bf16x8*)(lds + so + (((i) & 1) ? kb1 : kb0) + (((i) & 7) >> 1) * 512 + ((i) >> 3) * 8192))
#define VRD(i) (fr[(i) & 7] = trfrag(lds + so + trb + ((i) >> 2) * 4096 + ((i) & 3) * 512))
#define PFR(ks) __builtin_bit_cast(bf16x8, (u32x4){pw[4 * (ks)], pw[4 * (ks) + 1], pw[4 * (ks) + 2], pw[4 * (ks) + 3]})
#define EXP2(S, i, W) do { S[i] = __builtin_amdgcn_exp2f(S[i]); S[(i) + 1] = __builtin_amdgcn_exp2f(S[(i) + 1]); l += S[i]; lb += S[(i) + 1]; W = cvtpk(S[i], S[(i) + 1]); PIN(W); PIN(l); PIN(lb); } while (0)
    WAIT_TILE();
#pragma unroll
    for (int i = 0; i < 8; ++i) KRD(i);
    SBAR();
#pragma unroll
    for (int i = 0; i < 8; ++i) { s0 = MFMA32(fr[i], qf[i], i == 0 ? (f32x16){} : s0); KRD(i + 8); MEMFENCE(); SBAR(); }
    NEXT_TILE();
#pragma unroll
    for (int i = 0; i < 8; ++i) s1 = MFMA32(fr[i], qf[i], i == 0 ? (f32x16){} : s1);
    PIN(s0); PIN(s1);
    SBAR();
    WAIT_TILE();
#pragma unroll
    for (int i = 0; i < 8; ++i) KRD(i);
    MEMFENCE(); SBAR();
    {
        float a = max3f(s0[0], s0[1], s1[0]), c = max3f(s0[2], s0[3], s1[1]);
        a = max3f(a, s1[2], s1[3]);
#pragma unroll
        for (int i = 4; i < 16; i += 4) {
            a = max3f(a, s0[i], s0[i + 1]); c = max3f(c, s0[i + 2], s0[i + 3]);
            a = max3f(a, s1[i], s1[i + 1]); c = max3f(c, s1[i + 2], s1[i + 3]);
        }
        const float rm = xhalf_max(__builtin_fmaxf(a, c));
#pragma unroll
        for (int i = 0; i < 16; ++i) { negm[i] = -rm; s0[i] -= rm; s1[i] -= rm; }
#pragma unroll
        for (int i = 0; i < 16; i += 2) EXP2(s0, i, pw[i >> 1]);
        SBAR();
    }
    for (int t = 1; t < NKV; ++t) {
#pragma unroll
        for (int i = 0; i < 8; ++i) {
            s0 = MFMA32(fr[i], qf[i], i == 0 ? negm : s0);
            KRD(i + 8); MEMFENCE();
            EXP2(s1, 2 * i, pw[8 + i]);
            SBAR();
        }
        NEXT_TILE();
        WAIT_TILE();
#pragma unroll
        for (int i = 0; i < 8; ++i) {
            s1 = MFMA32(fr[i], qf[i], i == 0 ? negm : s1);
            VRD(i); MEMFENCE();
            SBAR();
        }
        float a, c;
        o[0] = MFMA32(fr[0], PFR(0), o[0]); VRD(8); MEMFENCE();
        a = max3f(s0[0], s0[1], s0[2]); c = max3f(s0[3], s0[4], s0[5]); a = max3f(a, s0[6], s0[7]); c = max3f(c, s0[8], s0[9]); PIN(a); PIN(c); SBAR();
        o[1] = MFMA32(fr[1], PFR(0), o[1]); VRD(9); MEMFENCE();
        a = max3f(a, s0[10], s0[11]); c = max3f(c, s0[12], s0[13]); a = max3f(a, s0[14], s0[15]); c = max3f(c, s1[0], s1[1]); PIN(a); PIN(c); SBAR();
        o[2] = MFMA32(fr[2], PFR(0), o[2]); VRD(10); MEMFENCE();
        a = max3f(a, s1[2], s1[3]); c = max3f(c, s1[4], s1[5]); a = max3f(a, s1[6], s1[7]); c = max3f(c, s1[8], s1[9]); PIN(a); PIN(c); SBAR();
        o[3] = MFMA32(fr[3], PFR(0), o[3]); VRD(11); MEMFENCE();
        a = max3f(a, s1[10], s1[11]); c = max3f(c, s1[12], s1[13]); a = max3f(a, s1[14], s1[15]); a = __builtin_fmaxf(a, c); PIN(a); SBAR();
        o[0] = MFMA32(fr[4], PFR(1), o[0]); VRD(12); MEMFENCE();
        const float rm = xhalf_max(a);
        PIN(o[0]);
        float al = 1.f;
        const bool resc = __any(rm > THR);
        if (__builtin_expect(resc, 0)) {
            const float dl = __builtin_fmaxf(rm, 0.f);
            al = __builtin_amdgcn_exp2f(-dl);
            const float nm = negm[0] - dl;
#pragma unroll
            for (int i = 0; i < 16; ++i) { negm[i] = nm; s0[i] -= dl; s1[i] -= dl; }
            l *= al; lb *= al;
        }
        SBAR();
        o[1] = MFMA32(fr[5], PFR(1), o[1]); VRD(13); MEMFENCE(); EXP2(s0, 0, pw[0]); SBAR();
        o[2] = MFMA32(fr[6], PFR(1), o[2]); VRD(14); MEMFENCE(); EXP2(s0, 2, pw[1]); SBAR();
        o[3] = MFMA32(fr[7], PFR(1), o[3]); VRD(15); MEMFENCE(); EXP2(s0, 4, pw[2]); SBAR();
        NEXT_TILE();
        o[0] = MFMA32(fr[0], PFR(2), o[0]); EXP2(s0, 6, pw[3]); SBAR();
        o[1] = MFMA32(fr[1], PFR(2), o[1]); EXP2(s0, 8, pw[4]); SBAR();
        o[2] = MFMA32(fr[2], PFR(2), o[2]); EXP2(s0, 10, pw[5]); SBAR();
        o[3] = MFMA32(fr[3], PFR(2), o[3]); EXP2(s0, 12, pw[6]); PIN(o[3]); SBAR();
        wait1(f_kv, kvl, t < NKV - 1 ? (unsigned)(jt + 1) : 0u);
        KRD(0); KRD(1); KRD(2); KRD(3); MEMFENCE(); SBAR();
        o[0] = MFMA32(fr[4], PFR(3), o[0]); KRD(4); MEMFENCE(); EXP2(s0, 14, pw[7]); SBAR();
        o[1] = MFMA32(fr[5], PFR(3), o[1]); KRD(5); MEMFENCE(); SBAR();
        o[2] = MFMA32(fr[6], PFR(3), o[2]); KRD(6); MEMFENCE(); SBAR();
        o[3] = MFMA32(fr[7], PFR(3), o[3]); KRD(7); MEMFENCE(); SBAR();
        PIN(o[0]); PIN(o[1]); PIN(o[2]); PIN(o[3]);
        if (__builtin_expect(resc, 0)) {
#pragma unroll
            for (int d = 0; d < 4; ++d)
#pragma unroll
                for (int i = 0; i < 16; ++i) o[d][i] *= al;
        }
        SBAR();
    }
#pragma unroll
    for (int i = 0; i < 8; ++i) EXP2(s1, 2 * i, pw[8 + i]);
    SBAR();
    l = xhalf_sum(l + lb);
    PIN(l);
    WAIT_TILE();
#pragma unroll
    for (int i = 0; i < 8; ++i) VRD(i);
    MEMFENCE(); SBAR();
#pragma unroll
    for (int i = 0; i < 8; ++i) { o[i & 3] = MFMA32(fr[i], PFR(i >> 2), o[i & 3]); VRD(i + 8); MEMFENCE(); SBAR(); }
#pragma unroll
    for (int i = 0; i < 8; ++i) o[i & 3] = MFMA32(fr[i], PFR(2 + (i >> 2)), o[i & 3]);
    PIN(o[3]);
    RT(t4);
    NEXT_TILE();
    const __amdgpu_buffer_rsrc_t rws = __builtin_amdgcn_make_buffer_rsrc((void*)ws, 0, WS_END, 0x00020000);
    const float inv = 1.0f / l;
    bf16x8 ob[4][8];
#pragma unroll
    for (int d = 0; d < 4; ++d) { ob[0][2 * d] = pack8(o[d], 0, inv); ob[0][2 * d + 1] = pack8(o[d], 8, inv); }
#pragma unroll
    for (int ks = 0; ks < 8; ++ks)
        __builtin_amdgcn_raw_buffer_store_b128(__builtin_bit_cast(u32x4, ob[0][ks]), rws, lane * 16, WS_O + ((b * NH + head) * 3 + wave) * 8192 + ks * 1024, 16);
    SBAR();
    int ob0 = trb, ob1 = trb + 5 * TILE; PIN(ob0); PIN(ob1);
#define ORD(dst, m, ks, t) (dst = trfrag(lds + (((NST + (m)) % NSL) < 5 ? ob0 + ((NST + (m)) % NSL) * TILE : ob1 + (((NST + (m)) % NSL) - 5) * TILE) + (ks) * 4096 + (t) * 512))
    f32x16 pa[4];
    unsigned wl = 0;
    wait4(f_wq, wl, (unsigned)(NWQ + 2));
    const u64 tag = *(volatile LAS const u64*)f_tag;
#pragma unroll
    for (int n = 0; n < 8; ++n) ORD(fr[n], 0, n >> 2, n & 3);
    MEMFENCE(); SBAR();
    static_for<0, 16>([&](auto ic) {
        constexpr int n = decltype(ic)::v, nn = n + 8;
        pa[n & 3] = MFMA32(fr[n & 7], ob[0][n >> 2], n < 4 ? (f32x16){} : pa[n & 3]); PIN(pa[n & 3]);
        ORD(fr[n & 7], nn >> 4, (nn >> 2) & 3, nn & 3); MEMFENCE(); SBAR();
    });
    wait_vm<0>();
    if (lane == 0) {
        const unsigned old = __hip_atomic_fetch_add((LAS unsigned*)f_cnt, 1u, __ATOMIC_RELAXED, __HIP_MEMORY_SCOPE_WORKGROUP);
        if (old == 2u) st_agent(ws + WS_FO + (b * 8 + grp) * 128 + mem * 8, tag);
    }
    RT(t1);
    poll_tags(ws + WS_FO + (b * 8 + grp) * 128 + (lane & 3) * 8, tag);
    RT(t2);
#pragma unroll
    for (int k = 1; k < 4; ++k)
#pragma unroll
        for (int ks = 0; ks < 8; ++ks)
            ob[k][ks] = __builtin_bit_cast(bf16x8, __builtin_amdgcn_raw_buffer_load_b128(rws, lane * 16, WS_O + ((b * NH + grp * 4 + ((mem + k) & 3)) * 3 + wave) * 8192 + ks * 1024, 16));
    SBAR();
    wait4(f_wq, wl, (unsigned)(NWQ + 8));
    const int pbase = WS_P + ((b * 4 + mem) * 64 + grp) * 3072 + wave * 1024;
    static_for<16, 128>([&](auto ic) {
        constexpr int n = decltype(ic)::v, nn = n + 8;
        constexpr int t = n < 32 ? (n & 3) : (n - 32) / 24, e = n < 32 ? (n >> 2) : 8 + (n - 32) % 24;
        pa[t] = MFMA32(fr[n & 7], ob[e >> 3][e & 7], pa[t]); PIN(pa[t]);
        if constexpr (nn < 128) {
            constexpr int t2 = nn < 32 ? (nn & 3) : (nn - 32) / 24, e2 = nn < 32 ? (nn >> 2) : 8 + (nn - 32) % 24;
            ORD(fr[n & 7], e2 >> 2, e2 & 3, t2); MEMFENCE();
        }
        if constexpr (n >= 56 + 3 && (n - 32) % 24 == 3) {
            st_sc1(pack8(pa[t - 1], 0, 1.f), rws, lane * 16, pbase + (2 * (t - 1)) * 8 * 3072);
            st_sc1(pack8(pa[t - 1], 8, 1.f), rws, lane * 16, pbase + (2 * (t - 1) + 1) * 8 * 3072);
        }
        SBAR();
    });
    st_sc1(pack8(pa[3], 0, 1.f), rws, lane * 16, pbase + 6 * 8 * 3072);
    st_sc1(pack8(pa[3], 8, 1.f), rws, lane * 16, pbase + 7 * 8 * 3072);
    [[maybe_unused]] unsigned long long t5 = 0, t6 = 0, t7 = 0, t8 = 0;
    RT(t5);
    wait_vm<0>();
    if (lane == 0) {
        const unsigned old = __hip_atomic_fetch_add((LAS unsigned*)(f_cnt + 4), 1u, __ATOMIC_RELAXED, __HIP_MEMORY_SCOPE_WORKGROUP);
        if (old == 2u) st_agent(ws + WS_FP + (b * 4 + mem) * 128 + grp * 8, tag);
    }
    RT(t6);
    poll_tags(ws + WS_FP + (b * 4 + mem) * 128 + (lane & 7) * 8, tag);
    RT(t7);
    u32x4 pp[8];
#pragma unroll
    for (int g = 0; g < 8; ++g) pp[g] = __builtin_amdgcn_raw_buffer_load_b128(rws, lane * 16, WS_P + ((b * 4 + mem) * 64 + grp * 8 + g) * 3072 + wave * 1024, 16);
    float acc[8] = {};
#pragma unroll
    for (int g = 0; g < 8; ++g)
#pragma unroll
        for (int d = 0; d < 4; ++d) { acc[2 * d] += __uint_as_float(pp[g][d] << 16); acc[2 * d + 1] += __uint_as_float(pp[g][d] & 0xffff0000u); }
    const int qrow = b * SQ + wave * 32 + 8 * (r & 3) + (r >> 2);
    *(u32x4*)(out + (size_t)qrow * DM + mem * 128 + grp * 16 + h * 8) = (u32x4){cvtpk(acc[0], acc[1]), cvtpk(acc[2], acc[3]), cvtpk(acc[4], acc[5]), cvtpk(acc[6], acc[7])};
    RT(t8);
    if (wave == 0) { PUT(0, t0); PUT(1, t3); PUT(2, t4); PUT(10, t1); PUT(11, t2); PUT(12, t5); PUT(13, t6); PUT(14, t7); PUT(3, t8); }
}

extern "C" void kernel_launch(void* const* d_in, const int* in_sizes, int n_in, void* d_out, int out_size, void* d_ws,
                              size_t ws_size, hipStream_t stream) {
    (void)hipFuncSetAttribute((const void*)attn_kernel, hipFuncAttributeMaxDynamicSharedMemorySize, LDS_BYTES);
    hipLaunchKernelGGL(attn_kernel, dim3(NB * NH), dim3(512), LDS_BYTES, stream, (const u16*)d_in[0], (const u16*)d_in[1], (const u16*)d_in[2],
                       (const u16*)d_in[3], (const u16*)d_in[4], (u16*)d_out, (char*)d_ws);
}
```

```cpp
#include <hip/hip_runtime.h>
#include <hip/hip_bf16.h>

typedef short bf16x8 __attribute__((ext_vector_type(8)));
typedef float f32x16 __attribute__((ext_vector_type(16)));
typedef float f32x2 __attribute__((ext_vector_type(2)));
typedef __bf16 bf16x2_t __attribute__((ext_vector_type(2)));
typedef unsigned u32x4 __attribute__((ext_vector_type(4)));
typedef short v4i16_t __attribute__((ext_vector_type(4)));
typedef unsigned short u16;
typedef unsigned long long u64;
#define LAS __attribute__((address_space(3)))
#define GAS __attribute__((address_space(1)))

constexpr int NB = 8, SQ = 96, DM = 512, NH = 32, SK = 1024, DH = 128, HD = NH * DH;
constexpr int TILE = 16384;
constexpr int NSL = 9;
constexpr int LDS_F = NSL * TILE;
constexpr int LDS_BYTES = LDS_F + 512;
constexpr int NWQ = 8;
constexpr int NKV = SK / 64;
constexpr int NST = 2 * NKV;
constexpr float QSCALE = 0.088388347648318440f * 1.4426950408889634f;
constexpr float PLIMIT = 4096.0f;
#define NWS 6
#define WN 18
#define XEARLY 2
#define TRIG 1
#define WO1 4
#define WO2 6
#define FLAGAT 8
#define KEEP 2
#define POLLAT 24
constexpr int wslot(int i) { return NSL - NWS + (i + 8 * NWS - NWQ) % NWS; }
static_assert(2 * NWS >= NWQ && NWS < NSL, "late wq tiles are published only after all of them have been requested");
constexpr int WS_EPOCH = 0;
constexpr int WS_FO = 4096;
constexpr int WS_FQ = 65536;
constexpr int WS_O = 262144;
constexpr int WS_P = WS_O + NB * NH * 24576;
constexpr int WS_END = WS_P + NB * 4 * 64 * 3072;
static_assert(WS_FO + NB * 8 * 3 * 128 <= WS_FQ && WS_FQ + NB * 4 * 8 * 3 * 128 <= WS_O, "flag areas");
constexpr unsigned SPIN_CAP = 1u << 22;

#define MFMA32(a, b, c) __builtin_amdgcn_mfma_f32_32x32x16_bf16((a), (b), (c), 0, 0, 0)
#define BAR() asm volatile("s_waitcnt lgkmcnt(0)\n\ts_barrier" ::: "memory")
#define SBAR() __builtin_amdgcn_sched_barrier(0)
#define PIN(x) asm volatile("" : "+v"(x))
#define MEMFENCE() asm volatile("" ::: "memory")

__device__ __forceinline__ unsigned cvtpk(float lo, float hi) {
    f32x2 v = {lo, hi};
    return __builtin_bit_cast(unsigned, __builtin_convertvector(v, bf16x2_t));
}
__device__ __forceinline__ bf16x8 pack8(const f32x16& a, int base, float s) {
    u32x4 w = {cvtpk(a[base + 0] * s, a[base + 1] * s), cvtpk(a[base + 2] * s, a[base + 3] * s),
               cvtpk(a[base + 4] * s, a[base + 5] * s), cvtpk(a[base + 6] * s, a[base + 7] * s)};
    return __builtin_bit_cast(bf16x8, w);
}
__device__ __forceinline__ bf16x8 trfrag(LAS const char* p) {
    v4i16_t lo = __builtin_amdgcn_ds_read_tr16_b64_v4i16((LAS v4i16_t*)p);
    v4i16_t hi = __builtin_amdgcn_ds_read_tr16_b64_v4i16((LAS v4i16_t*)(p + 256));
    return (bf16x8){lo[0], lo[1], lo[2], lo[3], hi[0], hi[1], hi[2], hi[3]};
}
__device__ __forceinline__ float max3f(float a, float b, float c) { return __builtin_fmaxf(__builtin_fmaxf(a, b), c); }
__device__ __forceinline__ float xhalf_max(float m) {
    auto rr = __builtin_amdgcn_permlane32_swap(__float_as_uint(m), __float_as_uint(m), false, false);
    return __builtin_fmaxf(__uint_as_float(rr[0]), __uint_as_float(rr[1]));
}
__device__ __forceinline__ float xhalf_sum(float m) {
    auto rr = __builtin_amdgcn_permlane32_swap(__float_as_uint(m), __float_as_uint(m), false, false);
    return __uint_as_float(rr[0]) + __uint_as_float(rr[1]);
}
template <int I> struct IC { static constexpr int v = I; };
template <int I, int N, class F> __device__ __forceinline__ void static_for(F&& f) {
    if constexpr (I < N) { f(IC<I>{}); static_for<I + 1, N>(f); }
}
template <int N> __device__ __forceinline__ void wait_vm() { asm volatile("s_waitcnt vmcnt(%0)" ::"i"(N) : "memory"); }
__device__ __forceinline__ unsigned lds_ld(LAS const char* p) { return __builtin_amdgcn_readfirstlane(*(volatile LAS const unsigned*)p); }
__device__ __forceinline__ void lds_st(LAS char* p, unsigned v) { MEMFENCE(); *(volatile LAS unsigned*)p = v; MEMFENCE(); }
__device__ __forceinline__ unsigned min3w(LAS const char* p) {
    const u32x4 v = *(volatile LAS const u32x4*)p;
    const unsigned m = v[0] < v[1] ? v[0] : v[1];
    return __builtin_amdgcn_readfirstlane(m < v[2] ? m : v[2]);
}
__device__ __forceinline__ unsigned min4v(u32x4 v) {
    const unsigned m = v[0] < v[1] ? v[0] : v[1], n = v[2] < v[3] ? v[2] : v[3];
    return __builtin_amdgcn_readfirstlane(m < n ? m : n);
}
__device__ __forceinline__ void wait4(LAS const char* p, unsigned& cached, unsigned need) {
    MEMFENCE();
    while (cached < need) cached = min4v(*(volatile LAS const u32x4*)p);
    MEMFENCE();
}
__device__ __forceinline__ void wait1(LAS const char* p, unsigned& cached, unsigned need) {
    MEMFENCE();
    while (cached < need) cached = lds_ld(p);
    MEMFENCE();
}
__device__ __forceinline__ u64 ld_agent(const char* p) { return __hip_atomic_load((GAS const u64*)p, __ATOMIC_RELAXED, __HIP_MEMORY_SCOPE_AGENT); }
__device__ __forceinline__ void st_agent(char* p, u64 v) { __hip_atomic_store((GAS u64*)p, v, __ATOMIC_RELAXED, __HIP_MEMORY_SCOPE_AGENT); }
__device__ __forceinline__ void poll_tags(const char* p, u64 tag) {
#pragma unroll 1
    for (unsigned spins = 0; spins < SPIN_CAP; ++spins) {
        if (__all(ld_agent(p) == tag)) break;
        __builtin_amdgcn_s_sleep(1);
    }
    __builtin_amdgcn_fence(__ATOMIC_ACQUIRE, "wavefront");
    MEMFENCE();
}
__device__ __forceinline__ void st_sc1(bf16x8 v, __amdgpu_buffer_rsrc_t rs, int vo, int so) {
    __builtin_amdgcn_raw_buffer_store_b128(__builtin_bit_cast(u32x4, v), rs, vo, so, 16);
    asm volatile("s_nop 1" :: "v"(v) : "memory");
}
#define DMA(rs, dst, vo, so, imm, aux) __builtin_amdgcn_raw_ptr_buffer_load_lds((rs), (LAS void*)(dst), 16, (vo), (so), (imm), (aux))

__global__ __launch_bounds__(512, 2) void gqa_block_kernel(const u16* __restrict__ x, const u16* __restrict__ kc,
                                                      const u16* __restrict__ vc, const u16* __restrict__ wq,
                                                      const u16* __restrict__ wo, u16* __restrict__ out, char* ws) {
    extern __shared__ __attribute__((aligned(16))) char smem[];
    LAS char* lds = (LAS char*)smem;
    LAS char* f_prog = lds + LDS_F;
    LAS char* f_wq = lds + LDS_F + 16;
    LAS char* f_kv = lds + LDS_F + 32;
    LAS char* f_go = lds + LDS_F + 36;
    LAS char* f_tag = lds + LDS_F + 48;
    const int tid = threadIdx.x, lane = tid & 63;
    const int wave = __builtin_amdgcn_readfirstlane(tid >> 6);
    const int bid = blockIdx.x;
    const int grp = bid & 7, mem = (bid >> 3) & 3, head = grp * 4 + mem, b = bid >> 5;
    const int rot = b + 2 * (head & 3);

    const int lrow = (lane >> 2) & 7, lcol = (lane >> 5) * 64 + (lane & 3) * 16;
    const __amdgpu_buffer_rsrc_t rx = __builtin_amdgcn_make_buffer_rsrc((void*)(x + (size_t)(b * SQ + (wave < 3 ? wave : 0) * 32) * DM), 0, 32 * DM * 2, 0x00020000);
    const __amdgpu_buffer_rsrc_t rw = __builtin_amdgcn_make_buffer_rsrc((void*)(wq + head * DH), 0, (DM * HD - head * DH) * 2, 0x00020000);
    const int vx = lrow * (DM * 2) + lcol, vw = lrow * (HD * 2) + lcol, lw = wave - 4;
    u32x4 xr[NWQ][4];
    auto ldx = [&](int i) {
        const int so = ((i + rot) & 7) * 128;
#pragma unroll
        for (int u = 0; u < 4; ++u) xr[i][u] = __builtin_amdgcn_raw_buffer_load_b128(rx, vx, so + u * 8 * (DM * 2), 0);
        SBAR();
    };
    auto wdma = [&](int i) {
        const int so = (((i + rot) & 7) * 64 + lw * 16) * (HD * 2);
#pragma unroll
        for (int p = 0; p < 4; ++p) DMA(rw, lds + lw * 4096 + wslot(i) * TILE + p * 1024, vw, so + (p >> 1) * 8 * (HD * 2) + (p & 1) * 128, 0, 0);
    };
    if (wave >= 4) wdma(0);
    else if (wave < 3) { ldx(0); ldx(1); }
    else if (lane < 16) ((LAS unsigned*)(lds + LDS_F))[lane] = 0u;
    BAR();

    if (wave == 3) {
        constexpr int BIAS = 2048;
        const size_t kvoff = ((size_t)(b * NH + head)) * SK * DH;
        const __amdgpu_buffer_rsrc_t rk = __builtin_amdgcn_make_buffer_rsrc((char*)(kc + kvoff) - BIAS, 0, SK * DH * 2 + BIAS, 0x00020000);
        const __amdgpu_buffer_rsrc_t rv = __builtin_amdgcn_make_buffer_rsrc((char*)(vc + kvoff) - BIAS, 0, SK * DH * 2 + BIAS, 0x00020000);
        int vk[4], vv[2];
#pragma unroll
        for (int j = 0; j < 4; ++j)
            vk[j] = (((lane >> 4) & 1) * 8 + ((lane >> 2) & 3)) * 256 + (lane >> 5) * 64 + (((lane & 3) ^ ((j & 2) + ((lane >> 4) & 1))) * 16)
                    + BIAS + (j >> 1) * 1024 + (j & 1) * 128 - j * 1024;
#pragma unroll
        for (int j = 0; j < 2; ++j) vv[j] = ((lane >> 2) & 7) * 256 + (lane >> 5) * 64 + (lane & 3) * 16 + BIAS + j * (128 - 1024);
        while (lds_ld(f_go) == 0u) __builtin_amdgcn_s_sleep(2);
        int pub = 0, lo = 0; unsigned P = 0;
        auto publish = [&](int n) {
            if (n > pub) {
                pub = n; lds_st(f_kv, (unsigned)pub);
            }
        };
        for (int j = 0; j < NST; ++j) {
            const unsigned need = j < NSL - NWS ? 0u : (unsigned)j;
            while (P < need) P = min3w(f_prog);
            MEMFENCE();
            LAS char* dst = lds + lo;
            const bool isk = (j == 0) || ((j & 1) && j != 31);
            const int so = (isk ? (j + 1) >> 1 : (j == 31 ? 15 : (j >> 1) - 1)) * TILE;
            const __amdgpu_buffer_rsrc_t rs = isk ? rk : rv;
            const int v0 = isk ? vk[0] : vv[0], v1 = isk ? vk[1] : vv[1], v2 = isk ? vk[2] : vv[0], v3 = isk ? vk[3] : vv[1];
#pragma unroll
            for (int q = 0; q < 4; ++q) {
                if (2 * q < KEEP) { DMA(rs, dst + q * 4096, v0, so + q * 4096, 0, 0); DMA(rs, dst + q * 4096, v1, so + q * 4096, 1024, 0); }
                else { DMA(rs, dst + q * 4096, v0, so + q * 4096, 0, 2); DMA(rs, dst + q * 4096, v1, so + q * 4096, 1024, 2); }
                if (2 * q + 1 < KEEP) { DMA(rs, dst + q * 4096, v2, so + q * 4096, 2048, 0); DMA(rs, dst + q * 4096, v3, so + q * 4096, 3072, 0); }
                else { DMA(rs, dst + q * 4096, v2, so + q * 4096, 2048, 2); DMA(rs, dst + q * 4096, v3, so + q * 4096, 3072, 2); }
                wait_vm<WN>();
                if (((4 * q + 4 + 64 - WN) & 15) < 4) publish((16 * j + 4 * q + 4 - WN) >> 4);
            }
            lo = (lo == (NSL - 1) * TILE) ? 0 : lo + TILE;
        }
        lds_st(f_go, 2u);
        static_assert(WN <= 32, "drain ladder");
        wait_vm<16>(); publish(NST - 1);
        wait_vm<0>(); publish(NST);
        return;
    }
    if (wave >= 4) {
        LAS char* dst = lds + lw * 4096;
        LAS char* myf = f_wq + lw * 4;
        const __amdgpu_buffer_rsrc_t ro = __builtin_amdgcn_make_buffer_rsrc((void*)(wo + (size_t)grp * 512 * DM + mem * 128), 0, (512 * DM - mem * 128) * 2, 0x00020000);
        const int vo = ((lane >> 2) & 7) * (DM * 2) + (lane >> 5) * 64 + (lane & 3) * 16;
        u64 tag = 0;
        if (lw == 3) tag = ld_agent(ws + WS_EPOCH);
        wait_vm<0>(); lds_st(myf, 1u);
        static_assert(TRIG >= 1, "trigger");
        static_for<1, NWS>([&](auto ic) {
            constexpr int i = decltype(ic)::v;
            wdma(i);
            if constexpr (i == TRIG) { if (lw == 0) lds_st(f_go, 1u); }
        });
        static_for<1, NWS>([&](auto ic) { constexpr int i = decltype(ic)::v; wait_vm<4 * (NWS - 1 - i)>(); lds_st(myf, (unsigned)(i + 1)); });
        static_for<NWS, NWQ>([&](auto ic) {
            constexpr int i = decltype(ic)::v;
            while (min3w(f_prog) < (unsigned)(i - NWS + 1)) __builtin_amdgcn_s_sleep(1);
            MEMFENCE();
            wdma(i);
        });
        static_for<NWS, NWQ>([&](auto ic) { constexpr int i = decltype(ic)::v; wait_vm<4 * (NWQ - 1 - i)>(); lds_st(myf, (unsigned)(i + 1)); });
        if (lw == 3) {
            tag = tag * 0x9E3779B97F4A7C15ull + 0x632BE59BD9B4E019ull;
            *(volatile LAS u64*)f_tag = tag;
        }
        while (lds_ld(f_go) < 2u) __builtin_amdgcn_s_sleep(8);
        unsigned P = 0;
        static_for<0, 8>([&](auto ic) {
            constexpr int m = decltype(ic)::v;
            constexpr unsigned need = m < WO1 ? NST + m : (m < WO2 ? NWQ + NST + 1 : NWQ + NST + 2);
            if constexpr (m == WO1) { wait_vm<4 * (WO1 - 2)>(); lds_st(myf, (unsigned)(NWQ + 2)); wait_vm<0>(); lds_st(myf, (unsigned)(NWQ + WO1)); }
            if constexpr (m == WO2 && WO2 > WO1) { wait_vm<0>(); lds_st(myf, (unsigned)(NWQ + WO2)); }
            while (P < need) { P = min3w(f_prog); if (P < need) __builtin_amdgcn_s_sleep(1); }
            MEMFENCE();
            const int so = ((((mem + (m >> 1)) & 3) * 2 + (m & 1)) * 64 + lw * 16) * (DM * 2);
#pragma unroll
            for (int p = 0; p < 4; ++p) DMA(ro, dst + ((NST + m) % NSL) * TILE + p * 1024, vo, so + (p >> 1) * 8 * (DM * 2) + (p & 1) * 128, 0, 0);
        });
        static_assert(WO1 >= 4 && WO1 <= WO2 && WO1 < 8, "publish ladder");
        if constexpr (WO2 < 6 || WO2 == 8) { wait_vm<8>(); lds_st(myf, (unsigned)(NWQ + 6)); }
        wait_vm<0>(); lds_st(myf, (unsigned)(NWQ + 8));
        if (bid == 0 && lw == 3) {
#pragma unroll 1
            for (int k = 0; k < 4; ++k) poll_tags(ws + WS_FO + lane * 3 * 128 + k * 8, tag);
            if (lane == 0) st_agent(ws + WS_EPOCH, tag);
        }
        return;
    }
    const int r = lane & 31, h = lane >> 5;
    const int p4 = lane & 3, sw = ((p4 & 1) << 1) | (p4 >> 1);
    const int trb = h * 2048 + ((lane >> 2) & 3) * 64 + ((lane >> 4) & 1) * 32 + sw * 8;
    const int kb0 = (r >> 3) * 2048 + (r & 7) * 64 + ((h ^ ((r >> 2) & 3)) * 16), kb1 = kb0 ^ 32;
    LAS char* myprog = f_prog + wave * 4;

    bf16x8 qf[8];
    {
#define XSEL(Q, M, d0, k0, o0, d1, k1, o1, d2, k2, o2, d3, k3, o3) \
        asm volatile("s_mov_b64 vcc, %12\n\t" \
            "v_cndmask_b32_dpp %0, %5, %4, vcc quad_perm:" Q " row_mask:0xf bank_mask:0xf\n\t" \
            "v_cndmask_b32_dpp %1, %7, %6, vcc quad_perm:" Q " row_mask:0xf bank_mask:0xf\n\t" \
            "v_cndmask_b32_dpp %2, %9, %8, vcc quad_perm:" Q " row_mask:0xf bank_mask:0xf\n\t" \
            "v_cndmask_b32_dpp %3, %11, %10, vcc quad_perm:" Q " row_mask:0xf bank_mask:0xf" \
            : "=&v"(d0), "=&v"(d1), "=&v"(d2), "=&v"(d3) \
            : "v"(k0), "v"(o0), "v"(k1), "v"(o1), "v"(k2), "v"(o2), "v"(k3), "v"(o3), "s"(M) : "vcc")
        u32x4 xt[4];
        auto xstep = [&](int i, int st) {
            const unsigned long long E0 = 0x5555555555555555ull, O0 = 0xAAAAAAAAAAAAAAAAull, E1 = 0x3333333333333333ull, O1 = 0xCCCCCCCCCCCCCCCCull;
            u32x4* X = xr[i];
            if (st == 0) XSEL("[1,0,3,2]", E0, xt[0][0], X[0][0], X[1][0], xt[0][1], X[0][1], X[1][1], xt[0][2], X[0][2], X[1][2], xt[0][3], X[0][3], X[1][3]);
            if (st == 1) XSEL("[1,0,3,2]", O0, xt[1][0], X[1][0], X[0][0], xt[1][1], X[1][1], X[0][1], xt[1][2], X[1][2], X[0][2], xt[1][3], X[1][3], X[0][3]);
            if (st == 2) XSEL("[1,0,3,2]", E0, xt[2][0], X[2][0], X[3][0], xt[2][1], X[2][1], X[3][1], xt[2][2], X[2][2], X[3][2], xt[2][3], X[2][3], X[3][3]);
            if (st == 3) XSEL("[1,0,3,2]", O0, xt[3][0], X[3][0], X[2][0], xt[3][1], X[3][1], X[2][1], xt[3][2], X[3][2], X[2][2], xt[3][3], X[3][3], X[2][3]);
            if (st == 4) XSEL("[2,3,0,1]", E1, X[0][0], xt[0][0], xt[2][0], X[0][1], xt[0][1], xt[2][1], X[0][2], xt[0][2], xt[2][2], X[0][3], xt[0][3], xt[2][3]);
            if (st == 5) XSEL("[2,3,0,1]", O1, X[2][0], xt[2][0], xt[0][0], X[2][1], xt[2][1], xt[0][1], X[2][2], xt[2][2], xt[0][2], X[2][3], xt[2][3], xt[0][3]);
            if (st == 6) XSEL("[2,3,0,1]", E1, X[1][0], xt[1][0], xt[3][0], X[1][1], xt[1][1], xt[3][1], X[1][2], xt[1][2], xt[3][2], X[1][3], xt[1][3], xt[3][3]);
            if (st == 7) XSEL("[2,3,0,1]", O1, X[3][0], xt[3][0], xt[1][0], X[3][1], xt[3][1], xt[1][1], X[3][2], xt[3][2], xt[1][2], X[3][3], xt[3][3], xt[1][3]);
        };
#pragma unroll
        for (int i = 2; i < NWQ; ++i) ldx(i);
#pragma unroll
        for (int i = 0; i < XEARLY; ++i)
#pragma unroll
            for (int st = 0; st < 8; ++st) xstep(i, st);
        f32x16 qa[4] = {};
        bf16x8 fw[8];
        int wb0 = trb + h * 6144 + TILE, wb1 = wb0 + 4 * TILE; PIN(wb0); PIN(wb1);
#define WRD(n) (fw[(n) & 7] = trfrag(lds + (wslot((n) >> 4) < 5 ? wb0 : wb1) + ((wslot((n) >> 4) - 1) & 3) * TILE + (((n) >> 2) & 3) * 2048 + ((n) & 3) * 512))
        unsigned wl = 0; u32x4 pf4;
        wait4(f_wq, wl, 1u);
#pragma unroll
        for (int n = 0; n < 8; ++n) WRD(n);
        MEMFENCE(); SBAR();
        static_for<0, 128>([&](auto ic) {
            constexpr int n = decltype(ic)::v;
            qa[n & 3] = MFMA32(fw[n & 7], __builtin_bit_cast(bf16x8, xr[n >> 4][(n >> 2) & 3]), qa[n & 3]);
            PIN(qa[n & 3]);
            constexpr int nn = n + 8, ti = n >> 4, ph = n & 15;
            if constexpr (nn < 128) {
                if constexpr ((nn & 15) == 0) {
                    lds_st(myprog, (unsigned)(nn >> 4));
                    wl = min4v(pf4);
                    wait4(f_wq, wl, (unsigned)((nn >> 4) + 1));
                }
                WRD(nn); MEMFENCE();
                if constexpr (nn == 127) lds_st(myprog, (unsigned)NWQ);
            }
            constexpr int SPX = (16 * (NWQ - 1)) / (8 * (NWQ - XEARLY)), xs_ = n / SPX;
            if constexpr (n % SPX == 0 && xs_ < 8 * (NWQ - XEARLY)) { static_assert((XEARLY + xs_ / 8) * 16 > n + 2, "late"); xstep(XEARLY + xs_ / 8, xs_ % 8); }
            if constexpr ((n & 3) == 3 && n < 116) __builtin_amdgcn_s_waitcnt(0xC87F);
            if constexpr (ph == 3 && ti + 1 < NWQ) { pf4 = *(volatile LAS const u32x4*)f_wq; MEMFENCE(); }
            SBAR();
        });
#undef WRD
#pragma unroll
        for (int t = 0; t < 4; ++t) { qf[2 * t] = pack8(qa[t], 0, QSCALE); qf[2 * t + 1] = pack8(qa[t], 8, QSCALE); }
    }

    f32x16 o[4] = {};
    f32x16 negm, s0, s1;
    float l = 0.f, nm = 0.f;
    unsigned kvl = 0;
    unsigned pwa[16], pwb[16];
    bf16x8 fr[8];
    int so = 0, jt = 0;
#define NEXT_TILE() do { ++jt; lds_st(myprog, (unsigned)(NWQ + jt)); so = (so == (NSL - 1) * TILE) ? 0 : so + TILE; } while (0)
#define WAIT_TILE() wait1(f_kv, kvl, (unsigned)(jt + 1))
#define KRD(i) (fr[(i) & 7] = *(LAS const bf16x8*)(lds + so + (((i) & 1) ? kb1 : kb0) + (((i) & 7) >> 1) * 512 + ((i) >> 3) * 8192))
#define VRD(i) (fr[(i) & 7] = trfrag(lds + so + trb + ((i) >> 2) * 4096 + ((i) & 3) * 512))
#define PFR(P, ks) __builtin_bit_cast(bf16x8, (u32x4){P[4 * (ks)], P[4 * (ks) + 1], P[4 * (ks) + 2], P[4 * (ks) + 3]})
#define MFMA_HEAD(D, A, B) asm volatile("v_mfma_f32_32x32x16_bf16 %0, %1, %2, %3" : "=&v"(D) : "v"(A), "v"(B), "v"(negm))
#define LGKM4() __builtin_amdgcn_s_waitcnt(0xC47F)
#define LGKM8() __builtin_amdgcn_s_waitcnt(0xC87F)
    auto rescale = [&](bool first, unsigned (&PN)[16], float& ts) {
        float a = max3f(s0[0], s0[1], s1[0]), c = max3f(s0[2], s0[3], s1[1]);
        a = max3f(a, s1[2], s1[3]);
#pragma unroll
        for (int i = 4; i < 16; i += 4) {
            a = max3f(a, s0[i], s0[i + 1]); c = max3f(c, s0[i + 2], s0[i + 3]);
            a = max3f(a, s1[i], s1[i + 1]); c = max3f(c, s1[i + 2], s1[i + 3]);
        }
        const float rm = xhalf_max(__builtin_fmaxf(a, c));
        const float dl = first ? rm : __builtin_fmaxf(rm, 0.f);
        nm -= dl;
#pragma unroll
        for (int i = 0; i < 16; ++i) negm[i] = nm;
        PIN(negm);
        ts = 0.f;
#pragma unroll
        for (int e = 0; e < 8; ++e) {
            const float x0 = __builtin_amdgcn_exp2f(s0[2 * e] - dl), x1 = __builtin_amdgcn_exp2f(s0[2 * e + 1] - dl);
            const float y0 = __builtin_amdgcn_exp2f(s1[2 * e] - dl), y1 = __builtin_amdgcn_exp2f(s1[2 * e + 1] - dl);
            ts += (x0 + x1) + (y0 + y1);
            PN[e] = cvtpk(x0, x1); PN[8 + e] = cvtpk(y0, y1);
        }
        return __builtin_amdgcn_exp2f(-dl);
    };
#define EXP1(PN, e) do { const float x_ = __builtin_amdgcn_exp2f((e) < 16 ? s0[(e) & 15] : s1[(e) & 15]); \
        if constexpr ((e) & 1) { tb += x_; PN[(e) >> 1] = cvtpk(xe, x_); PIN(PN[(e) >> 1]); PIN(tb); } else { ta += x_; xe = x_; PIN(xe); PIN(ta); } } while (0)
    auto step = [&](unsigned (&PP)[16], unsigned (&PN)[16]) {
        float ta = 0.f, tb = 0.f, xe = 0.f;
        static_for<0, 32>([&](auto ic) {
            constexpr int g = decltype(ic)::v, i = g & 7;
            if constexpr (g == 0) MFMA_HEAD(s0, fr[0], qf[0]);
            else if constexpr (g < 8) { s0 = MFMA32(fr[i], qf[i], s0); PIN(s0); }
            else if constexpr (g == 8) MFMA_HEAD(s1, fr[0], qf[0]);
            else if constexpr (g < 16) { s1 = MFMA32(fr[i], qf[i], s1); PIN(s1); }
            else { o[i & 3] = MFMA32(fr[i], PFR(PP, (g - 16) >> 2), o[i & 3]); PIN(o[i & 3]); }
            SBAR();
            if constexpr (g < 8) KRD(g + 8); else if constexpr (g < 24) VRD(g - 8); else KRD(g - 24);
            MEMFENCE();
            constexpr auto first = [](int q) { return q <= 10 ? 0 : (q <= 24 ? ((q - 10) * 9) / 7 : (q >= 31 ? 32 : 18 + (q - 24) * 2)); };
            static_assert(g >= 18 || first(g + 1) <= 16, "s1 is not complete yet");
            static_for<first(g), first(g + 1)>([&](auto ie) { constexpr int e = decltype(ie)::v; EXP1(PN, e); });
            if constexpr (g == 7 || g == 23) { NEXT_TILE(); WAIT_TILE(); }
            if constexpr (g == 3 || g == 27 || g == 31) LGKM4();
            if constexpr (g == 7) __builtin_amdgcn_s_waitcnt(0xC57F);
            if constexpr (g == 11 || g == 15 || g == 19) LGKM8();
            if constexpr (g == 23) __builtin_amdgcn_s_waitcnt(0xC97F);
            SBAR();
        });
        float ts = ta + tb;
        if (__builtin_expect(__any(!(ts <= PLIMIT)), 0)) {
            const float al = rescale(false, PN, ts);
            l *= al;
#pragma unroll
            for (int d = 0; d < 4; ++d)
#pragma unroll
                for (int i = 0; i < 16; ++i) o[d][i] *= al;
        }
        l += ts;
        SBAR();
    };
    WAIT_TILE();
#pragma unroll
    for (int i = 0; i < 8; ++i) KRD(i);
    MEMFENCE(); SBAR();
#pragma unroll
    for (int i = 0; i < 8; ++i) { s0 = MFMA32(fr[i], qf[i], i == 0 ? (f32x16){} : s0); PIN(s0); KRD(i + 8); MEMFENCE(); SBAR(); }
    NEXT_TILE(); WAIT_TILE();
#pragma unroll
    for (int i = 0; i < 8; ++i) { s1 = MFMA32(fr[i], qf[i], i == 0 ? (f32x16){} : s1); PIN(s1); KRD(i); MEMFENCE(); SBAR(); }
    (void)rescale(true, pwa, l);
    SBAR();
    for (int t = 1; t < NKV - 1; t += 2) { step(pwa, pwb); step(pwb, pwa); }
    float al15 = 1.f; bool resc15;
    {
        float ta = 0.f, tb = 0.f, xe = 0.f;
        static_for<0, 16>([&](auto ic) {
            constexpr int g = decltype(ic)::v, i = g & 7;
            if constexpr (g == 0) MFMA_HEAD(s0, fr[0], qf[0]);
            else if constexpr (g < 8) { s0 = MFMA32(fr[i], qf[i], s0); PIN(s0); }
            else if constexpr (g == 8) MFMA_HEAD(s1, fr[0], qf[0]);
            else { s1 = MFMA32(fr[i], qf[i], s1); PIN(s1); }
            SBAR();
            if constexpr (g < 8) { KRD(g + 8); MEMFENCE(); }
            if constexpr (g == 7) NEXT_TILE();
            if constexpr (g >= 10) static_for<(g - 10) * 2, (g - 9) * 2>([&](auto ie) { constexpr int e = decltype(ie)::v; EXP1(pwb, e); });
            SBAR();
        });
        static_for<12, 32>([&](auto ie) { constexpr int e = decltype(ie)::v; EXP1(pwb, e); });
        float ts = ta + tb;
        resc15 = __any(!(ts <= PLIMIT));
        if (__builtin_expect(resc15, 0)) { al15 = rescale(false, pwb, ts); l *= al15; }
        l += ts;
    }
    float inv = 1.0f / xhalf_sum(l);
    PIN(inv);
    const __amdgpu_buffer_rsrc_t rws = __builtin_amdgcn_make_buffer_rsrc((void*)ws, 0, WS_END, 0x00020000);
    bf16x8 ob[4][8];
    WAIT_TILE();
#pragma unroll
    for (int i = 0; i < 8; ++i) VRD(i);
    MEMFENCE(); SBAR();
#pragma unroll
    for (int i = 0; i < 8; ++i) { o[i & 3] = MFMA32(fr[i], PFR(pwa, i >> 2), o[i & 3]); PIN(o[i & 3]); VRD(i + 8); MEMFENCE(); SBAR(); }
    NEXT_TILE();
#pragma unroll
    for (int i = 0; i < 8; ++i) { o[i & 3] = MFMA32(fr[i], PFR(pwa, 2 + (i >> 2)), o[i & 3]); PIN(o[i & 3]); }
    if (__builtin_expect(resc15, 0)) {
#pragma unroll
        for (int d = 0; d < 4; ++d)
#pragma unroll
            for (int i = 0; i < 16; ++i) o[d][i] *= al15;
    }
    SBAR();
    WAIT_TILE();
#define VRD2(n) (fr[(n) & 7] = trfrag(lds + so + trb + ((n) & 3) * 4096 + ((n) >> 2) * 512))
#pragma unroll
    for (int n = 0; n < 8; ++n) VRD2(n);
    MEMFENCE(); SBAR();
    const int obase = WS_O + ((b * NH + head) * 3 + wave) * 8192;
    static_for<0, 16>([&](auto ic) {
        constexpr int n = decltype(ic)::v, d = n >> 2;
        o[d] = MFMA32(fr[n & 7], PFR(pwb, n & 3), o[d]); PIN(o[d]);
        if constexpr (n < 8) { VRD2(n + 8); MEMFENCE(); }
        if constexpr (d > 0 && (n & 3) >= 2) { ob[0][2 * (d - 1) + (n & 1)] = pack8(o[d - 1], 8 * (n & 1), inv); st_sc1(ob[0][2 * (d - 1) + (n & 1)], rws, lane * 16, obase + (2 * (d - 1) + (n & 1)) * 1024); }
        SBAR();
    });
    ob[0][6] = pack8(o[3], 0, inv); st_sc1(ob[0][6], rws, lane * 16, obase + 6 * 1024);
    ob[0][7] = pack8(o[3], 8, inv); st_sc1(ob[0][7], rws, lane * 16, obase + 7 * 1024);
    SBAR();
    lds_st(myprog, (unsigned)(NWQ + NST + 1));
    f32x16 pa[4];
    unsigned wl = 0;
    int wb0 = trb, wb1 = trb + 5 * TILE; PIN(wb0); PIN(wb1);
    constexpr auto o_e = [](int n) { return n < 96 ? n >> 2 : 24 + (n & 7); };
    constexpr auto o_t = [](int n) { return n < 96 ? n & 3 : (n - 96) >> 3; };
#define ORD(n) (fr[(n) & 7] = trfrag(lds + (o_e(n) < 16 ? wb1 : wb0) + (o_e(n) & 15) * 4096 + o_t(n) * 512))
    wait4(f_wq, wl, (unsigned)(NWQ + 2));
    const u64 tag = *(volatile LAS const u64*)f_tag;
    char* fol = ws + WS_FO + ((b * 8 + grp) * 3 + wave) * 128;
#pragma unroll
    for (int n = 0; n < 8; ++n) ORD(n);
    MEMFENCE(); SBAR();
    const int pbase = WS_P + ((b * 4 + mem) * 64 + grp) * 3072 + wave * 1024;
    const int pvo = (r >> 3) * 2 * 8 * 3072 + h * 128 + (r & 7) * 16;
    constexpr auto pso = [](int f) { return (f >> 2) * 8 * 3072 + (f & 3) * 256; };
    static_for<0, 128>([&](auto ic) {
        constexpr int n = decltype(ic)::v, nn = n + 8, t = o_t(n), e = o_e(n);
        pa[t] = MFMA32(fr[n & 7], ob[e >> 3][e & 7], n < 4 ? (f32x16){} : pa[t]); PIN(pa[t]);
        if constexpr (n == FLAGAT) {
            wait_vm<0>();
            if (lane == 0) st_agent(fol + mem * 8, tag);
        }
        if constexpr (n == POLLAT) {
            poll_tags(fol + (((lane & 3) == mem ? mem + 1 : lane) & 3) * 8, tag);
#pragma unroll
            for (int k = 1; k < 4; ++k)
#pragma unroll
                for (int ks = 0; ks < 8; ++ks)
                    ob[k][ks] = __builtin_bit_cast(bf16x8, __builtin_amdgcn_raw_buffer_load_b128(rws, lane * 16, WS_O + ((b * NH + grp * 4 + ((mem + k) & 3)) * 3 + wave) * 8192 + ks * 1024, 16));
            lds_st(myprog, (unsigned)(NWQ + NST + 2));
        }
        if constexpr (nn < 128) {
            if constexpr ((nn & 31) == 0) wait4(f_wq, wl, (unsigned)(NWQ + 2 + nn / 16));
            ORD(nn); MEMFENCE();
        }
        if constexpr (n >= 104 && (n & 7) == 3) st_sc1(pack8(pa[t - 1], 0, 1.f), rws, pvo, pbase + pso(2 * (t - 1)));
        if constexpr (n >= 104 && (n & 7) == 5) st_sc1(pack8(pa[t - 1], 8, 1.f), rws, pvo, pbase + pso(2 * (t - 1) + 1));
        if constexpr ((n & 3) == 3 && n < 116) LGKM8();
        SBAR();
    });
    st_sc1(pack8(pa[3], 0, 1.f), rws, pvo, pbase + pso(6));
    st_sc1(pack8(pa[3], 8, 1.f), rws, pvo, pbase + pso(7));
    wait_vm<0>();
    if (lane < 8) st_agent(ws + WS_FQ + (((b * 4 + mem) * 8 + lane) * 3 + wave) * 128 + grp * 8, tag);
    const char* fpl = ws + WS_FQ + (((b * 4 + mem) * 8 + grp) * 3 + wave) * 128;
    poll_tags(fpl + (((lane & 7) == grp ? grp + 1 : lane) & 7) * 8, tag);
    u32x4 pp[8];
#pragma unroll
    for (int g = 0; g < 8; ++g) pp[g] = __builtin_amdgcn_raw_buffer_load_b128(rws, lane * 16, WS_P + ((b * 4 + mem) * 64 + grp * 8 + g) * 3072 + wave * 1024, 16);
    float acc[8] = {};
#pragma unroll
    for (int g = 0; g < 8; ++g)
#pragma unroll
        for (int d = 0; d < 4; ++d) { acc[2 * d] += __uint_as_float(pp[g][d] << 16); acc[2 * d + 1] += __uint_as_float(pp[g][d] & 0xffff0000u); }
    const __amdgpu_buffer_rsrc_t rout = __builtin_amdgcn_make_buffer_rsrc((void*)out, 0, NB * SQ * DM * 2, 0x00020000);
    u32x4 res = {cvtpk(acc[0], acc[1]), cvtpk(acc[2], acc[3]), cvtpk(acc[4], acc[5]), cvtpk(acc[6], acc[7])};
    const int src = ((lane & 7) * 8 + (lane >> 3)) * 4;
#pragma unroll
    for (int d = 0; d < 4; ++d) res[d] = __builtin_amdgcn_ds_bpermute(src, res[d]);
    const int rc = (grp >> 1) * 8 + (lane >> 3);
    const int qrow = b * SQ + wave * 32 + 8 * (rc & 3) + (rc >> 2);
    __builtin_amdgcn_raw_buffer_store_b128(res, rout, (qrow * DM + mem * 128 + (grp & 1) * 64 + (lane & 7) * 8) * 2, 0, 16);
}

extern "C" void kernel_launch(void* const* d_in, const int* in_sizes, int n_in, void* d_out, int out_size, void* d_ws,
                              size_t ws_size, hipStream_t stream) {
    (void)hipFuncSetAttribute((const void*)gqa_block_kernel, hipFuncAttributeMaxDynamicSharedMemorySize, LDS_BYTES);
    hipLaunchKernelGGL(gqa_block_kernel, dim3(NB * NH), dim3(512), LDS_BYTES, stream, (const u16*)d_in[0], (const u16*)d_in[1], (const u16*)d_in[2],
                       (const u16*)d_in[3], (const u16*)d_in[4], (u16*)d_out, (char*)d_ws);
}
```
